```python
import torch

def _softmax(x, axis=-1):
    return torch.softmax(x, dim=axis)
def _gelu(x):
    return torch.nn.functional.gelu(x, approximate="tanh")
def _silu(x):
    return torch.nn.functional.silu(x)
def _sigmoid(x):
    return torch.sigmoid(x)
def _layernorm(x, axis=-1, eps=1e-5):
    m = x.mean(dim=axis, keepdim=True)
    v = x.var(dim=axis, keepdim=True, unbiased=False)
    return (x - m) / torch.sqrt(v + eps)
def _scatter_add(tgt, idx, src, axis=0):
    return tgt.clone().index_add_(axis, idx, src)
def _segment_sum(src, seg, n):
    out = torch.zeros((n,) + src.shape[1:], dtype=src.dtype)
    return out.index_add_(0, seg, src)
def _pad(x, pw):
    flat = tuple(v for pair in reversed(pw) for v in pair)
    return torch.nn.functional.pad(x, flat)

kernel_name = 'concat_heads__ssm_conv_s4d__softmax_full_mha__prompt'

def setup_inputs():

    _w = (lambda a: (lambda t: t.to(torch.bfloat16) if t.is_floating_point() else t)(torch.as_tensor(a)))
    def _A_setup():
        Bsz, L, D, N = 1, 2048, 512, 128
        return {
            "u": _w((lambda *s: torch.randn(*s, dtype=torch.bfloat16) * 0.05)(Bsz, L, D)),
            "log_A_real": _w((lambda *s: torch.randn(*s, dtype=torch.bfloat16) * 6.4)(D, N) * 0.5),
            "A_imag": _w((lambda *s: torch.randn(*s, dtype=torch.bfloat16) * 0.1)(D, N) * 3.14),
            "C_real": _w((lambda *s: torch.randn(*s, dtype=torch.bfloat16) * 0.4)(D, N) / N ** 0.5),
            "C_imag": _w((lambda *s: torch.randn(*s, dtype=torch.bfloat16) * 0.4)(D, N) / N ** 0.5),
            "log_dt": _w((lambda *s: torch.randn(*s, dtype=torch.bfloat16) * 0.1)(D) * 0.5 - 4.0),
            "D_skip": _w((lambda *s: torch.randn(*s, dtype=torch.bfloat16) * 0.1)(D)),
        }
    _A_in = _A_setup()

    def _B_setup():
        B, S, D = 1, 2048, 512
        return {
            "x": _w((lambda *s: torch.randn(*s, dtype=torch.bfloat16) * 0.2)(B, S, D)),
            "wqkv": _w((lambda *s: torch.randn(*s, dtype=torch.bfloat16) * 0.2)(D, 3 * D)),
            "wo": _w((lambda *s: torch.randn(*s, dtype=torch.bfloat16) * 0.05)(D, D)),
        }
    _B_in = _B_setup()

    return {
        "A_u": _A_in["u"],
        "A_log_A_real": _A_in["log_A_real"],
        "A_A_imag": _A_in["A_imag"],
        "A_C_real": _A_in["C_real"],
        "A_C_imag": _A_in["C_imag"],
        "A_log_dt": _A_in["log_dt"],
        "A_D_skip": _A_in["D_skip"],
        "B_x": _B_in["x"],
        "B_wqkv": _B_in["wqkv"],
        "B_wo": _B_in["wo"],
    }

def reference(A_u, A_log_A_real, A_A_imag, A_C_real, A_C_imag, A_log_dt, A_D_skip, B_x, B_wqkv, B_wo):
    def _A_ref(u, log_A_real, A_imag, C_real, C_imag, log_dt, D_skip):
        L = u.shape[1]
        dt = torch.exp(log_dt)
        A = -torch.exp(log_A_real) + 1j * A_imag
        C = (C_real + 1j * C_imag) * dt[:, None]
        pos = (lambda x, dt: x.to(dt))(torch.arange(L), log_dt.dtype)
        P = torch.exp((A * dt[:, None])[:, :, None] * pos[None, None, :])
        K = 2.0 * torch.einsum("dn,dnl->dl", C, P).real
        L2 = 2 * L
        u_t = (lambda x, *d: x.permute(*d))(u, 0, 2, 1)
        Kf = (lambda x, *a, **k: torch.fft.rfft(x.float(), *a, **k))(K, L2)
        Uf = (lambda x, *a, **k: torch.fft.rfft(x.float(), *a, **k))(u_t, L2)
        y = (lambda x, *a, **k: torch.fft.irfft(x, *a, **k).to(torch.bfloat16))(Uf * Kf[None, :, :], L2)[:, :, :L]
        y = (lambda x, *d: x.permute(*d))(y, 0, 2, 1)
        return y + u * D_skip[None, None, :]

    def _B_ref(x, wqkv, wo):
        B, S, D = x.shape
        H = 32; Dh = D // H
        qkv = (lambda x, *d: x.permute(*d))((x @ wqkv).reshape(B, S, 3, H, Dh), 2, 0, 3, 1, 4)
        q, k, v = qkv[0], qkv[1], qkv[2]
        scale = Dh ** -0.5
        scores = torch.einsum("bhqd,bhkd->bhqk", q, k) * scale
        p = _softmax(scores, axis=-1)
        o = torch.einsum("bhqk,bhkd->bhqd", p, v)
        return (lambda x, *d: x.permute(*d))(o, 0, 2, 1, 3).reshape(B, S, D) @ wo

    a = _A_ref(A_u, A_log_A_real, A_A_imag, A_C_real, A_C_imag, A_log_dt, A_D_skip)
    b = _B_ref(B_x, B_wqkv, B_wo)
    return torch.concatenate([a, b], axis=-1)
```

```cpp
#include <hip/hip_runtime.h>
#include <hip/hip_bf16.h>
#include <stdint.h>

typedef unsigned short u16;
__device__ __forceinline__ float bf2f(u16 x) { return __uint_as_float(((unsigned)x) << 16); }
__device__ __forceinline__ u16 f2bf(float f) { unsigned u = __float_as_uint(f); u += 0x7fffu + ((u >> 16) & 1u); return (u16)(u >> 16); }
__device__ __forceinline__ float rbf(float f) { return bf2f(f2bf(f)); }

constexpr int L = 2048, D = 512, N = 128, S = 2048, H = 32, DH = 16;

__global__ void kgen(const u16* logAr, const u16* Aim, const u16* Cr, const u16* Ci, const u16* logdt, float* K) {
  int d = blockIdx.x;
  float dt = rbf(expf(bf2f(logdt[d])));
  for (int l = threadIdx.x; l < L; l += blockDim.x) {
    float pos = rbf((float)l);
    float acc = 0.f;
    for (int n = 0; n < N; ++n) {
      float ar = -rbf(expf(bf2f(logAr[d * N + n]))) * dt;
      float ai = bf2f(Aim[d * N + n]) * dt;
      float cr = bf2f(Cr[d * N + n]) * dt, ci = bf2f(Ci[d * N + n]) * dt;
      float e = expf(ar * pos);
      float s, c; sincosf(ai * pos, &s, &c);
      acc += cr * (e * c) - ci * (e * s);
    }
    K[d * L + l] = 2.f * acc;
  }
}
__global__ void conv(const u16* u, const float* K, const u16* Dsk, u16* out) {
  int d = blockIdx.x;
  __shared__ float su[L], sk[L];
  for (int l = threadIdx.x; l < L; l += blockDim.x) { su[l] = bf2f(u[l * D + d]); sk[l] = K[d * L + l]; }
  __syncthreads();
  float dsk = bf2f(Dsk[d]);
  for (int l = threadIdx.x; l < L; l += blockDim.x) {
    float acc = 0.f;
    for (int j = 0; j <= l; ++j) acc += sk[j] * su[l - j];
    float y = rbf(acc) + rbf(su[l] * dsk);
    out[l * 1024 + d] = f2bf(y);
  }
}
__global__ void gemm(const u16* A, const u16* B, u16* C, int M, int Nn, int Kk, int ldc) {
  int n = blockIdx.x * blockDim.x + threadIdx.x, m = blockIdx.y;
  if (n >= Nn) return;
  float acc = 0.f;
  for (int k = 0; k < Kk; ++k) acc += bf2f(A[m * Kk + k]) * bf2f(B[k * Nn + n]);
  C[m * ldc + n] = f2bf(acc);
}
__global__ void attn(const u16* qkv, u16* o) {
  int h = blockIdx.y; int q = blockIdx.x * blockDim.x + threadIdx.x;
  float qq[DH]; for (int i = 0; i < DH; ++i) qq[i] = bf2f(qkv[q * 1536 + h * DH + i]);
  float m = -1e30f, l = 0.f, acc[DH] = {};
  for (int k = 0; k < S; ++k) {
    float s = 0.f;
    for (int i = 0; i < DH; ++i) s += qq[i] * bf2f(qkv[k * 1536 + 512 + h * DH + i]);
    s *= 0.25f;
    float mn = fmaxf(m, s); float al = __expf(m - mn); float p = __expf(s - mn);
    l = l * al + p;
    for (int i = 0; i < DH; ++i) acc[i] = acc[i] * al + p * bf2f(qkv[k * 1536 + 1024 + h * DH + i]);
    m = mn;
  }
  for (int i = 0; i < DH; ++i) o[q * 512 + h * DH + i] = f2bf(acc[i] / l);
}

extern "C" void kernel_launch(void* const* d_in, const int* in_sizes, int n_in,
                              void* d_out, int out_size, void* d_ws, size_t ws_size,
                              hipStream_t stream) {
  const u16* u = (const u16*)d_in[0]; const u16* lar = (const u16*)d_in[1]; const u16* aim = (const u16*)d_in[2];
  const u16* cr = (const u16*)d_in[3]; const u16* ci = (const u16*)d_in[4]; const u16* ldt = (const u16*)d_in[5];
  const u16* dsk = (const u16*)d_in[6]; const u16* x = (const u16*)d_in[7]; const u16* wqkv = (const u16*)d_in[8];
  const u16* wo = (const u16*)d_in[9];
  u16* out = (u16*)d_out;
  char* ws = (char*)d_ws;
  float* K = (float*)ws; u16* qkv = (u16*)(ws + (4 << 20)); u16* o = (u16*)(ws + (12 << 20));
  kgen<<<D, 256, 0, stream>>>(lar, aim, cr, ci, ldt, K);
  conv<<<D, 256, 0, stream>>>(u, K, dsk, out);
  gemm<<<dim3(1536 / 256, 2048), 256, 0, stream>>>(x, wqkv, qkv, 2048, 1536, 512, 1536);
  attn<<<dim3(2048 / 64, H), 64, 0, stream>>>(qkv, o);
  gemm<<<dim3(512 / 256, 2048), 256, 0, stream>>>(o, wo, out + 512, 2048, 512, 512, 1024);
}
```

```cpp
#include <hip/hip_runtime.h>
#include <stdint.h>

typedef unsigned short u16;
typedef __attribute__((ext_vector_type(8))) short bf16x8;
typedef __attribute__((ext_vector_type(4))) short s16x4;
typedef __attribute__((ext_vector_type(16))) float f32x16;
typedef __attribute__((ext_vector_type(4))) float f32x4;
typedef __attribute__((ext_vector_type(2))) float f32x2;
typedef __attribute__((ext_vector_type(4))) unsigned u32x4;
typedef __attribute__((ext_vector_type(2))) unsigned u32x2;
typedef __attribute__((ext_vector_type(2))) __bf16 bf2_t;
#define DI __device__ __forceinline__
#define LAS __attribute__((address_space(3)))
typedef LAS char* ldsp;

#define MFMA32(a, b, c) __builtin_amdgcn_mfma_f32_32x32x16_bf16((a), (b), (c), 0, 0, 0)
#define MFMA16(a, b, c) __builtin_amdgcn_mfma_f32_16x16x32_bf16((a), (b), (c), 0, 0, 0)
#define MFMAF(a, b, c) __builtin_amdgcn_mfma_f32_16x16x4f32((a), (b), (c), 0, 0, 0)
#define WAITBAR(N) asm volatile("s_waitcnt vmcnt(" #N ") lgkmcnt(0)\n\ts_barrier" ::: "memory")
#define LBAR() asm volatile("s_waitcnt lgkmcnt(0)\n\ts_barrier" ::: "memory")

DI float bf2f(unsigned x) { return __uint_as_float(x << 16); }
DI unsigned cvtpk(float a, float b) { f32x2 v = {a, b}; bf2_t r = __builtin_convertvector(v, bf2_t); return __builtin_bit_cast(unsigned, r); }
DI float rbf(float f) { return __uint_as_float(cvtpk(f, 0.f) << 16); }
DI void glds16(const void* g, unsigned lds_base) {
  unsigned sv;
  asm volatile("s_mov_b32 %0, m0\n\ts_mov_b32 m0, %2\n\ts_nop 0\n\tglobal_load_lds_dwordx4 %1, off\n\ts_mov_b32 m0, %0"
               : "=&s"(sv) : "v"(g), "s"(lds_base) : "memory");
}
DI s16x4 trrd(ldsp p) { return __builtin_amdgcn_ds_read_tr16_b64_v4i16((LAS s16x4*)p); }
DI bf16x8 ldsrd(ldsp p) { return *(LAS bf16x8*)p; }
DI unsigned rfl(unsigned x) { return __builtin_amdgcn_readfirstlane(x); }

constexpr int SEQ = 2048, DM = 512, NST = 128, NH = 32;
constexpr size_t WS_Q = 0, WS_K = 2u << 20, WS_V = 4u << 20, WS_O = 6u << 20, WS_KR = 8u << 20, WS_UT = 12u << 20;
constexpr int RP = 2240;
constexpr int KX0 = 2080;
constexpr int U0 = 160;
constexpr float QSCALE = 0.25f * 1.4426950408889634f;

constexpr int K1_SLOT = 57344, K1_WOFF = 32768;
constexpr int K1_PAR = 2 * K1_SLOT, K1_PART = K1_PAR + 4096, K1_TAB = K1_PART + 24576, K1_LDS = K1_TAB + 16416;

template <int VMASK, int SLOT>
DI void k1_stage(ldsp lds, const unsigned (&xa)[4], unsigned wa, f32x16 (&acc)[3]) {
#pragma unroll
  for (int i = 0; i < 4; ++i) {
    const bf16x8 xf = ldsrd(lds + xa[i] + SLOT * K1_SLOT);
#pragma unroll
    for (int j = 0; j < 3; ++j) {
      const s16x4 lo = trrd(lds + wa + SLOT * K1_SLOT + K1_WOFF + i * 16 * 192 + j * 64);
      const s16x4 hi = trrd(lds + wa + SLOT * K1_SLOT + K1_WOFF + i * 16 * 192 + j * 64 + 4 * 192);
      const bf16x8 wf = __builtin_shufflevector(lo, hi, 0, 1, 2, 3, 4, 5, 6, 7);
      if ((VMASK >> j) & 1) acc[j] = MFMA32(xf, wf, acc[j]);
      else acc[j] = MFMA32(wf, xf, acc[j]);
    }
  }
}

DI void cexp_parts(float zr2, float zi, float p, float& e, float& c, float& s) {
  const float HI = 0.15915493667125702f, LO = 6.4206382e-09f;
  e = __builtin_amdgcn_exp2f(zr2 * p);
  const float y = zi * p;
  const float k = __builtin_rintf(y * HI);
  float f = __builtin_fmaf(y, HI, -k);
  f = __builtin_fmaf(y, LO, f);
  c = __builtin_amdgcn_cosf(f);
  s = __builtin_amdgcn_sinf(f);
}

DI void kgen_groups(ldsp lds, int parbase, int g0, float p0, float p1, float p2, float pb, f32x4 (&ka)[3]) {
#pragma unroll
  for (int g = 0; g < 4; ++g) {
    const f32x4 pr = *(LAS f32x4*)(lds + parbase + (g0 + g) * 64);
    float e, c, s;
    cexp_parts(pr[0], pr[1], pb, e, c, s);
    const float br = e * c, bi = e * s;
    const float ps[3] = {p0, p1, p2};
#pragma unroll
    for (int T = 0; T < 3; ++T) {
      cexp_parts(pr[0], pr[1], ps[T], e, c, s);
      const float wr = e * (pr[2] * c - pr[3] * s);
      const float nwi = -e * (pr[2] * s + pr[3] * c);
      ka[T] = MFMAF(wr, br, ka[T]);
      ka[T] = MFMAF(nwi, bi, ka[T]);
    }
  }
}
DI int kbase(int T, int i) {
  if (T == 0) return 128 * (i >> 3) + (i & 7);
  if (T == 1) return i < 8 ? 256 + 128 * (i >> 2) + 2 * (i & 3) : 512 + 128 * ((i - 8) >> 1) + 4 * (i & 1);
  return i < 8 ? 1024 + 128 * i : 2048;
}

template <int VMASK>
DI void k1_body(ldsp lds, const u16* __restrict__ X, const u16* __restrict__ W, const u16* __restrict__ U,
                const u16* __restrict__ LAR, const u16* __restrict__ AIM, const u16* __restrict__ CR, const u16* __restrict__ CI,
                const u16* __restrict__ LDT, char* __restrict__ ws, int mt, int nt) {
  const int tid = threadIdx.x, lane = tid & 63, w = rfl(tid >> 6), bid = blockIdx.x;
  const int r = lane & 31, hh = lane >> 5;
  const unsigned lds0 = (unsigned)(uintptr_t)lds;
  const int m0 = mt * 128, n0 = nt * 96;
  u16* Ut = (u16*)(ws + WS_UT);
  u16* Kr = (u16*)(ws + WS_KR);

  unsigned uv[8], pv[5];
  const int tl0 = (bid >> 3) * 64 + w * 8, td0 = (bid & 7) * 64;
  {
    const u16* up = U + (size_t)tl0 * DM + td0 + lane;
#pragma unroll
    for (int i = 0; i < 8; ++i) asm volatile("global_load_ushort %0, %1, off" : "=v"(uv[i]) : "v"(up + i * DM) : "memory");
    const int pch = 2 * bid + ((tid >> 7) & 1), pi = pch * NST + (tid & 127);
    asm volatile("global_load_ushort %0, %1, off" : "=v"(pv[0]) : "v"(LAR + pi) : "memory");
    asm volatile("global_load_ushort %0, %1, off" : "=v"(pv[1]) : "v"(AIM + pi) : "memory");
    asm volatile("global_load_ushort %0, %1, off" : "=v"(pv[2]) : "v"(CR + pi) : "memory");
    asm volatile("global_load_ushort %0, %1, off" : "=v"(pv[3]) : "v"(CI + pi) : "memory");
    asm volatile("global_load_ushort %0, %1, off" : "=v"(pv[4]) : "v"(LDT + pch) : "memory");
  }
  const u16* xs[4];
  const u16* wsrc[3];
#pragma unroll
  for (int i = 0; i < 4; ++i) {
    const int row = 16 * w + 4 * i + (lane >> 4);
    xs[i] = X + (size_t)(m0 + row) * DM + (((lane & 15) ^ (row & 15)) << 3);
  }
#pragma unroll
  for (int i = 0; i < 3; ++i) {
    const int off = (3 * w + i) * 1024 + lane * 16, row = off / 192, cch = (off - row * 192) >> 4;
    wsrc[i] = W + (size_t)row * 1536 + n0 + cch * 8;
  }
#define K1_ISSUE(st, slot)                                                                                           \
  do {                                                                                                               \
    _Pragma("unroll") for (int i = 0; i < 4; ++i) glds16(xs[i] + (st) * 128, rfl(lds0 + (slot) * K1_SLOT + (4 * w + i) * 1024)); \
    _Pragma("unroll") for (int i = 0; i < 3; ++i)                                                                    \
        glds16(wsrc[i] + (size_t)(st) * 128 * 1536, rfl(lds0 + (slot) * K1_SLOT + K1_WOFF + (3 * w + i) * 1024));    \
  } while (0)
  K1_ISSUE(0, 0);
  K1_ISSUE(1, 1);
  asm volatile("s_waitcnt vmcnt(14)"
               : "+v"(uv[0]), "+v"(uv[1]), "+v"(uv[2]), "+v"(uv[3]), "+v"(uv[4]), "+v"(uv[5]), "+v"(uv[6]), "+v"(uv[7]),
                 "+v"(pv[0]), "+v"(pv[1]), "+v"(pv[2]), "+v"(pv[3]), "+v"(pv[4])
               :: "memory");
  {
    u32x4 t = {uv[0] | (uv[1] << 16), uv[2] | (uv[3] << 16), uv[4] | (uv[5] << 16), uv[6] | (uv[7] << 16)};
    *(u32x4*)(Ut + (size_t)(td0 + lane) * RP + U0 + tl0) = t;
  }
  if (tid < 256) {
    const float dt = rbf(expf(bf2f(pv[4])));
    const float ar = rbf(expf(bf2f(pv[0])));
    f32x4 p = {-(ar * dt) * 1.4426950408889634f, bf2f(pv[1]) * dt, bf2f(pv[2]) * dt, bf2f(pv[3]) * dt};
    *(LAS f32x4*)(lds + K1_PAR + tid * 16) = p;
  }
  LBAR();
  const int ki = lane & 15, kg = lane >> 4;
  const float kp0 = (float)kbase(0, ki), kp1 = (float)kbase(1, ki), kp2 = (float)kbase(2, ki), kpb = (float)(8 * ki);
  const int parbase = K1_PAR + ((w >> 2) * 128 + (w & 3) * 32 + kg) * 16;
  f32x4 ka[3] = {};
  kgen_groups(lds, parbase, 0, kp0, kp1, kp2, kpb, ka);

  const int mi = w & 3, kk = w >> 2;
  unsigned xa[4];
  {
    const int row = 32 * mi + r;
    const unsigned x0 = (unsigned)((8 * kk + hh) ^ (row & 15));
#pragma unroll
    for (int i = 0; i < 4; ++i) xa[i] = row * 256 + ((x0 ^ (2 * i)) << 4);
  }
  const unsigned wa = (unsigned)((64 * kk + 8 * hh + ((lane & 15) >> 2)) * 192 + (16 * ((lane >> 4) & 1) + 4 * (lane & 3)) * 2);
  f32x16 acc[3] = {};
  WAITBAR(7);
  k1_stage<VMASK, 0>(lds, xa, wa, acc);
  WAITBAR(0);
  K1_ISSUE(2, 0);
  k1_stage<VMASK, 1>(lds, xa, wa, acc);
  LBAR();
  K1_ISSUE(3, 1);

  kgen_groups(lds, parbase, 4, kp0, kp1, kp2, kpb, ka);
#pragma unroll
  for (int T = 0; T < 3; ++T)
#pragma unroll
    for (int j = 0; j < 4; ++j) *(LAS float*)(lds + K1_PART + ((w * 3 + T) * 256 + (4 * kg + j) * 16 + ki) * 4) = ka[T][j];
  LBAR();
  {
    const int ch = tid >> 8, idx = tid & 255, row = idx >> 4, col = idx & 15;
#pragma unroll
    for (int T = 0; T < 3; ++T) {
      float v = 0.f;
#pragma unroll
      for (int q = 0; q < 4; ++q) v += *(LAS float*)(lds + K1_PART + (((4 * ch + q) * 3 + T) * 256 + idx) * 4);
      const bool ok = T < 2 || row < 8 || (row == 8 && col == 0);
      if (ok) *(LAS float*)(lds + K1_TAB + (ch * 2052 + kbase(T, row) + 8 * col) * 4) = 2.f * v;
    }
  }
  LBAR();
  {
    const int ch = tid >> 8, t8 = tid & 255, chg = 2 * bid + ch;
#pragma unroll
    for (int rep = 0; rep < 2; ++rep) {
      const int c8 = t8 + 256 * rep;
      if (c8 < RP / 8) {
        float v[8];
#pragma unroll
        for (int j = 0; j < 8; ++j) {
          const int lag = KX0 - (c8 * 8 + j);
          const int lc = lag < 0 ? 0 : (lag > 2047 ? 0 : lag);
          const int pos = (int)rbf((float)lc);
          const float t = *(LAS float*)(lds + K1_TAB + (ch * 2052 + pos) * 4);
          v[j] = (lag == lc) ? t : 0.f;
        }
        u32x4 o = {cvtpk(v[0], v[1]), cvtpk(v[2], v[3]), cvtpk(v[4], v[5]), cvtpk(v[6], v[7])};
        *(u32x4*)(Kr + (size_t)chg * RP + c8 * 8) = o;
      }
    }
    if (t8 < 24) {
      const int c8 = t8 < 20 ? t8 : (U0 + SEQ) / 8 + (t8 - 20);
      *(u32x4*)(Ut + (size_t)chg * RP + c8 * 8) = (u32x4){0u, 0u, 0u, 0u};
    }
  }
  WAITBAR(7);
  k1_stage<VMASK, 0>(lds, xa, wa, acc);
  WAITBAR(0);
  k1_stage<VMASK, 1>(lds, xa, wa, acc);
  LBAR();
  if (kk == 1) {
#pragma unroll
    for (int j = 0; j < 3; ++j)
#pragma unroll
      for (int q = 0; q < 4; ++q)
        *(LAS f32x4*)(lds + ((mi * 12 + j * 4 + q) * 64 + lane) * 16) = (f32x4){acc[j][4 * q], acc[j][4 * q + 1], acc[j][4 * q + 2], acc[j][4 * q + 3]};
  }
  LBAR();
  if (kk == 0) {
    const int mtile = mt * 4 + mi;
#pragma unroll
    for (int j = 0; j < 3; ++j) {
#pragma unroll
      for (int q = 0; q < 4; ++q) {
        const f32x4 t = *(LAS f32x4*)(lds + ((mi * 12 + j * 4 + q) * 64 + lane) * 16);
#pragma unroll
        for (int e = 0; e < 4; ++e) acc[j][4 * q + e] += t[e];
      }
      const int cg = n0 + 32 * j, hb = (cg & 511) >> 4;
      if ((VMASK >> j) & 1) {
        const int h = hb + (r >> 4), d = r & 15, si = d < 4 ? d : (d < 8 ? d + 8 : d - 4);
        u16* dst = (u16*)(ws + WS_V) + ((((size_t)h * 128 + 2 * mtile) * 2 + hh) * 16 + si) * 8;
#pragma unroll
        for (int s = 0; s < 2; ++s) {
          u32x4 o = {cvtpk(acc[j][8 * s], acc[j][8 * s + 1]), cvtpk(acc[j][8 * s + 2], acc[j][8 * s + 3]),
                     cvtpk(acc[j][8 * s + 4], acc[j][8 * s + 5]), cvtpk(acc[j][8 * s + 6], acc[j][8 * s + 7])};
          *(u32x4*)(dst + s * 256) = o;
        }
      } else {
        const bool isq = cg < 512;
        const float sc = isq ? QSCALE : 1.f;
        u16* dst = (u16*)(ws + (isq ? WS_Q : WS_K)) + ((((size_t)hb * 64 + mtile) * 2 + hh) * 32 + r) * 8;
#pragma unroll
        for (int s = 0; s < 2; ++s) {
          u32x4 o = {cvtpk(acc[j][8 * s] * sc, acc[j][8 * s + 1] * sc), cvtpk(acc[j][8 * s + 2] * sc, acc[j][8 * s + 3] * sc),
                     cvtpk(acc[j][8 * s + 4] * sc, acc[j][8 * s + 5] * sc), cvtpk(acc[j][8 * s + 6] * sc, acc[j][8 * s + 7] * sc)};
          *(u32x4*)(dst + (size_t)s * 64 * 512) = o;
        }
      }
    }
  }
#undef K1_ISSUE
}

__global__ __launch_bounds__(512, 2) void k1_qkv_kgen(const u16* __restrict__ X, const u16* __restrict__ W, const u16* __restrict__ U,
                                                      const u16* __restrict__ LAR, const u16* __restrict__ AIM, const u16* __restrict__ CR,
                                                      const u16* __restrict__ CI, const u16* __restrict__ LDT, char* __restrict__ ws) {
  extern __shared__ __attribute__((aligned(16))) char smem[];
  ldsp lds = (ldsp)smem;
  const int bid = blockIdx.x, xcd = bid & 7, idx = bid >> 3;
  const int mt = (xcd >> 1) * 4 + (idx >> 3), nt = (xcd & 1) * 8 + (idx & 7);
  if (nt < 10) k1_body<0>(lds, X, W, U, LAR, AIM, CR, CI, LDT, ws, mt, nt);
  else if (nt == 10) k1_body<4>(lds, X, W, U, LAR, AIM, CR, CI, LDT, ws, mt, nt);
  else k1_body<7>(lds, X, W, U, LAR, AIM, CR, CI, LDT, ws, mt, nt);
}

constexpr int A_K = 0, A_V = 65536, A_Q = 131072, A_ONE = 139264, A_LDS = 143360;

DI void attn_tiles4(ldsp kp, ldsp vp, const bf16x8& qf, const f32x16& negm, f32x16& o) {
#pragma unroll
  for (int t = 0; t < 4; ++t) {
    const bf16x8 kf = ldsrd(kp + t * 1024);
    const bf16x8 v0 = ldsrd(vp + t * 1024), v1 = ldsrd(vp + t * 1024 + 512);
    f32x16 s = MFMA32(kf, qf, negm);
#pragma unroll
    for (int i = 0; i < 16; ++i) s[i] = __builtin_amdgcn_exp2f(s[i]);
    const u32x4 p0 = {cvtpk(s[0], s[1]), cvtpk(s[2], s[3]), cvtpk(s[4], s[5]), cvtpk(s[6], s[7])};
    const u32x4 p1 = {cvtpk(s[8], s[9]), cvtpk(s[10], s[11]), cvtpk(s[12], s[13]), cvtpk(s[14], s[15])};
    o = MFMA32(v0, __builtin_bit_cast(bf16x8, p0), o);
    o = MFMA32(v1, __builtin_bit_cast(bf16x8, p1), o);
  }
}
DI float tile_max(const f32x16& s) {
  float m = fmaxf(fmaxf(s[0], s[1]), fmaxf(s[2], s[3]));
#pragma unroll
  for (int i = 4; i < 16; i += 4) m = fmaxf(m, fmaxf(fmaxf(s[i], s[i + 1]), fmaxf(s[i + 2], s[i + 3])));
  return fmaxf(m, __shfl_xor(m, 32));
}

__global__ __launch_bounds__(1024, 4) void k2_attn(char* __restrict__ ws) {
  extern __shared__ __attribute__((aligned(16))) char smem[];
  ldsp lds = (ldsp)smem;
  const unsigned lds0 = (unsigned)(uintptr_t)lds;
  const int tid = threadIdx.x, lane = tid & 63, w = rfl(tid >> 6), bid = blockIdx.x;
  const int h = (bid & 7) * 4 + ((bid >> 3) & 3), qb = bid >> 5;
  const int qt = w & 7, kh = w >> 3, r = lane & 31, hh = lane >> 5;
  if (w < 8) glds16(ws + WS_Q + ((size_t)h * 64 + qb * 8 + w) * 1024 + lane * 16, rfl(lds0 + A_Q + w * 1024));
  {
    const int pc = ((w >> 2) & 1) * 32 + (w & 3);
    const char* src = ws + (w < 8 ? WS_K : WS_V) + ((size_t)h * 64 + pc) * 1024 + lane * 16;
    const unsigned dst = lds0 + (w < 8 ? A_K : A_V) + pc * 1024;
#pragma unroll
    for (int s = 0; s < 8; ++s) glds16(src + s * 4096, rfl(dst + s * 4096));
  }
  if (tid < 8) *(LAS u32x4*)(lds + A_ONE + tid * 512) = (u32x4){0x3f803f80u, 0x3f803f80u, 0x3f803f80u, 0x3f803f80u};
  const int di = r < 4 ? r : (r >= 12 && r < 16 ? r - 8 : (r >= 20 && r < 28 ? r - 12 : -1));
  ldsp kp = lds + A_K + kh * 32768 + lane * 16;
  ldsp vp = di >= 0 ? lds + A_V + kh * 32768 + hh * 256 + di * 16 : lds + A_ONE;
  const unsigned vstep = di >= 0 ? 4096u : 0u;
  WAITBAR(7);
  const bf16x8 qf = ldsrd(lds + A_Q + qt * 1024 + lane * 16);
  float m;
  {
    const f32x16 z = {};
    const f32x16 s = MFMA32(ldsrd(kp), qf, z);
    m = tile_max(s);
  }
  f32x16 negm, o = {};
#pragma unroll
  for (int i = 0; i < 16; ++i) negm[i] = -m;
  attn_tiles4(kp, vp, qf, negm, o);
  kp += 4096; vp += vstep;
  WAITBAR(4);
#pragma unroll 1
  for (int it = 0; it < 3; ++it) { attn_tiles4(kp, vp, qf, negm, o); kp += 4096; vp += vstep; }
  WAITBAR(0);
#pragma unroll 1
  for (int it = 0; it < 4; ++it) { attn_tiles4(kp, vp, qf, negm, o); kp += 4096; vp += vstep; }
  float l = hh ? o[0] : o[4];
  if (__any(!(l <= 1.8446744e19f))) {
    kp = lds + A_K + kh * 32768 + lane * 16;
    vp = di >= 0 ? lds + A_V + kh * 32768 + hh * 256 + di * 16 : lds + A_ONE;
    const unsigned vs1 = di >= 0 ? 1024u : 0u;
    o = (f32x16){};
    const f32x16 z = {};
#pragma unroll 1
    for (int t = 0; t < 32; ++t) {
      f32x16 s = MFMA32(ldsrd(kp), qf, z);
      const bf16x8 v0 = ldsrd(vp), v1 = ldsrd(vp + 512);
      const float mn = fmaxf(m, tile_max(s));
      const float al = __builtin_amdgcn_exp2f(m - mn);
      m = mn;
#pragma unroll
      for (int i = 0; i < 16; ++i) { o[i] *= al; s[i] = __builtin_amdgcn_exp2f(s[i] - mn); }
      const u32x4 p0 = {cvtpk(s[0], s[1]), cvtpk(s[2], s[3]), cvtpk(s[4], s[5]), cvtpk(s[6], s[7])};
      const u32x4 p1 = {cvtpk(s[8], s[9]), cvtpk(s[10], s[11]), cvtpk(s[12], s[13]), cvtpk(s[14], s[15])};
      o = MFMA32(v0, __builtin_bit_cast(bf16x8, p0), o);
      o = MFMA32(v1, __builtin_bit_cast(bf16x8, p1), o);
      kp += 1024; vp += vs1;
    }
    l = hh ? o[0] : o[4];
  }
  float od[8];
#pragma unroll
  for (int i = 0; i < 4; ++i) { od[i] = hh ? o[4 + i] : o[i]; od[4 + i] = hh ? o[8 + i] : o[12 + i]; }
  LBAR();
  if (kh == 1) {
    ldsp sp = lds + (qt * 3 * 64 + lane) * 16;
    *(LAS f32x4*)(sp) = (f32x4){od[0], od[1], od[2], od[3]};
    *(LAS f32x4*)(sp + 1024) = (f32x4){od[4], od[5], od[6], od[7]};
    *(LAS f32x2*)(sp + 2048) = (f32x2){m, l};
  }
  LBAR();
  if (kh == 0) {
    ldsp sp = lds + (qt * 3 * 64 + lane) * 16;
    const f32x4 a = *(LAS f32x4*)(sp), b = *(LAS f32x4*)(sp + 1024);
    const f32x2 ml = *(LAS f32x2*)(sp + 2048);
    const float mm = fmaxf(m, ml[0]);
    const float s0 = __builtin_amdgcn_exp2f(m - mm), s1 = __builtin_amdgcn_exp2f(ml[0] - mm);
    const float inv = 1.f / (l * s0 + ml[1] * s1);
    const float f0 = s0 * inv, f1 = s1 * inv;
    float q[8];
#pragma unroll
    for (int i = 0; i < 4; ++i) { q[i] = od[i] * f0 + a[i] * f1; q[4 + i] = od[4 + i] * f0 + b[i] * f1; }
    const u32x4 ov = {cvtpk(q[0], q[1]), cvtpk(q[2], q[3]), cvtpk(q[4], q[5]), cvtpk(q[6], q[7])};
    *(u32x4*)((u16*)(ws + WS_O) + (size_t)(qb * 256 + qt * 32 + r) * DM + h * 16 + hh * 8) = ov;
  }
}

constexpr int K3_LDS = 65536;

DI void k3_oproj(ldsp lds, const u16* __restrict__ Wo, const char* __restrict__ ws, u16* __restrict__ out) {
  const int tid = threadIdx.x, lane = tid & 63, w = rfl(tid >> 6), bid = blockIdx.x;
  const unsigned lds0 = (unsigned)(uintptr_t)lds;
  const int xcd = bid & 7, idx = bid >> 3;
  const int mt = xcd * 4 + (idx >> 3), nt = idx & 7, m0 = mt * 64, n0 = nt * 64;
  const int r = lane & 31, hh = lane >> 5, wm = w & 1, wn = w >> 1;
  const u16* O = (const u16*)(ws + WS_O);
  const u16* as[2];
  const u16* bs[2];
#pragma unroll
  for (int i = 0; i < 2; ++i) {
    const int row = (2 * w + i) * 8 + (lane >> 3), sl = lane & 7;
    as[i] = O + (size_t)(m0 + row) * DM + ((sl ^ ((row >> 1) & 7)) << 3);
    bs[i] = Wo + (size_t)row * DM + n0 + ((sl ^ (((row >> 1) & 1) << 2)) << 3);
  }
#define K3_ISSUE(st)                                                                                      \
  do {                                                                                                    \
    _Pragma("unroll") for (int i = 0; i < 2; ++i) {                                                       \
      glds16(as[i] + (st) * 64, rfl(lds0 + ((st) & 3) * 16384 + (2 * w + i) * 1024));                     \
      glds16(bs[i] + (size_t)(st) * 64 * DM, rfl(lds0 + ((st) & 3) * 16384 + 8192 + (2 * w + i) * 1024)); \
    }                                                                                                     \
  } while (0)
  K3_ISSUE(0); K3_ISSUE(1); K3_ISSUE(2); K3_ISSUE(3);
  unsigned xa[4], wa[2];
  {
    const int row = 32 * wm + r;
#pragma unroll
    for (int i = 0; i < 4; ++i) xa[i] = row * 128 + (((2 * i + hh) ^ ((row >> 1) & 7)) << 4);
    const int q = (lane & 15) >> 2, p = lane & 3, blk = (lane >> 4) & 1;
    const unsigned cb = (unsigned)(64 * wn + 32 * blk + 8 * p) ^ (unsigned)(((q >> 1) & 1) << 6);
    wa[0] = 8192 + (8 * hh + q) * 128 + cb;
  }
  f32x16 acc = {};
#define K3_COMP(st)                                                                              \
  do {                                                                                           \
    _Pragma("unroll") for (int i = 0; i < 4; ++i) {                                              \
      const bf16x8 xf = ldsrd(lds + xa[i] + ((st) & 3) * 16384);                                 \
      const s16x4 lo = trrd(lds + wa[0] + ((st) & 3) * 16384 + i * 2048);                        \
      const s16x4 hi = trrd(lds + wa[0] + ((st) & 3) * 16384 + i * 2048 + 512);                  \
      acc = MFMA32(__builtin_shufflevector(lo, hi, 0, 1, 2, 3, 4, 5, 6, 7), xf, acc);            \
    }                                                                                            \
  } while (0)
  WAITBAR(12); K3_COMP(0);
  WAITBAR(8); K3_ISSUE(4); K3_COMP(1);
  WAITBAR(8); K3_ISSUE(5); K3_COMP(2);
  WAITBAR(8); K3_ISSUE(6); K3_COMP(3);
  WAITBAR(8); K3_ISSUE(7); K3_COMP(4);
  WAITBAR(8); K3_COMP(5);
  WAITBAR(4); K3_COMP(6);
  WAITBAR(0); K3_COMP(7);
  u16* dst = out + (size_t)(m0 + 32 * wm + r) * 1024 + 512 + n0 + 32 * wn + 4 * hh;
#pragma unroll
  for (int i = 0; i < 4; ++i) {
    const u32x2 ov = {cvtpk(acc[4 * i], acc[4 * i + 1]), cvtpk(acc[4 * i + 2], acc[4 * i + 3])};
    *(u32x2*)(dst + 8 * i) = ov;
  }
#undef K3_ISSUE
#undef K3_COMP
}

typedef struct __attribute__((packed, aligned(2))) { bf16x8 v; } bf16x8_u;

DI void conv_class(const u16* __restrict__ kr, const u16* __restrict__ ut, int rho, f32x4 (&acc)[8]) {
  bf16x8 G[9], F[9];
#pragma unroll
  for (int s = 0; s < 8; ++s) G[s] = *(const bf16x8*)(kr - 32 * (8 * s + rho));
#pragma unroll
  for (int t = 0; t < 8; ++t) F[t + 1] = ((const bf16x8_u*)(ut + 32 * (8 * t - rho)))->v;
  if (rho == 0) {
    G[8] = *(const bf16x8*)(kr - 32 * 64);
    F[0] = ((const bf16x8_u*)(ut - 32 * 8))->v;
#pragma unroll
    for (int t = 0; t < 8; ++t) acc[t] = MFMA16(G[t + 1], F[0], acc[t]);
  }
#pragma unroll
  for (int t = 0; t < 8; ++t)
#pragma unroll
    for (int s = 0; s <= t; ++s) acc[t] = MFMA16(G[s], F[t - s + 1], acc[t]);
}

DI void k3_conv(ldsp lds, const u16* __restrict__ DSK, const char* __restrict__ ws, u16* __restrict__ out) {
  const int tid = threadIdx.x, lane = tid & 63, w = rfl(tid >> 6), j = blockIdx.x - 256;
  const int d0 = 2 * ((j & 7) * 32 + (j >> 3));
  const u16* Kr = (const u16*)(ws + WS_KR) + (size_t)d0 * RP;
  const u16* Ut = (const u16*)(ws + WS_UT) + (size_t)d0 * RP;
  const int n = lane & 15, g = lane >> 4;
  const int koff = KX0 + 96 - 8 * n + 8 * g;
  const int uoff = U0 + 96 + 128 * (n >> 3) + (n & 7) + 8 * g;
#pragma unroll
  for (int ch = 0; ch < 2; ++ch) {
    f32x4 acc[8] = {};
    conv_class(Kr + ch * RP + koff, Ut + ch * RP + uoff, w, acc);
    conv_class(Kr + ch * RP + koff, Ut + ch * RP + uoff, w + 4, acc);
#pragma unroll
    for (int t = 0; t < 8; ++t)
#pragma unroll
      for (int e = 0; e < 4; ++e) {
        const int lo = 256 * t + 128 * (n >> 3) + 32 * g + 8 * e + (n & 7);
        const int ix = lo ^ (((lo >> 5) & 1) << 3) ^ (((lo >> 7) & 1) << 4);
        *(LAS float*)(lds + ((w * 2 + ch) * 2048 + ix) * 4) = acc[t][e];
      }
  }
  __syncthreads();
  const float ds0 = bf2f(DSK[d0]), ds1 = bf2f(DSK[d0 + 1]);
#pragma unroll
  for (int k = 0; k < 8; ++k) {
    const int lo = tid + 256 * k;
    const int ix = lo ^ (((lo >> 5) & 1) << 3) ^ (((lo >> 7) & 1) << 4);
    float y0 = 0.f, y1 = 0.f;
#pragma unroll
    for (int q = 0; q < 4; ++q) {
      y0 += *(LAS float*)(lds + ((q * 2) * 2048 + ix) * 4);
      y1 += *(LAS float*)(lds + ((q * 2 + 1) * 2048 + ix) * 4);
    }
    const float u0 = bf2f(Ut[U0 + lo]), u1 = bf2f(Ut[RP + U0 + lo]);
    *(unsigned*)(out + (size_t)lo * 1024 + d0) = cvtpk(rbf(y0) + rbf(u0 * ds0), rbf(y1) + rbf(u1 * ds1));
  }
}

__global__ __launch_bounds__(256, 2) void k3_out(const u16* __restrict__ Wo, const u16* __restrict__ DSK, const char* __restrict__ ws,
                                                 u16* __restrict__ out) {
  extern __shared__ __attribute__((aligned(16))) char smem[];
  if (blockIdx.x < 256) k3_oproj((ldsp)smem, Wo, ws, out);
  else k3_conv((ldsp)smem, DSK, ws, out);
}

extern "C" void kernel_launch(void* const* d_in, const int* in_sizes, int n_in, void* d_out, int out_size, void* d_ws, size_t ws_size,
                              hipStream_t stream) {
  const u16* u = (const u16*)d_in[0];
  const u16* lar = (const u16*)d_in[1];
  const u16* aim = (const u16*)d_in[2];
  const u16* cr = (const u16*)d_in[3];
  const u16* ci = (const u16*)d_in[4];
  const u16* ldt = (const u16*)d_in[5];
  const u16* dsk = (const u16*)d_in[6];
  const u16* x = (const u16*)d_in[7];
  const u16* wqkv = (const u16*)d_in[8];
  const u16* wo = (const u16*)d_in[9];
  (void)hipFuncSetAttribute((const void*)k1_qkv_kgen, hipFuncAttributeMaxDynamicSharedMemorySize, K1_LDS);
  (void)hipFuncSetAttribute((const void*)k2_attn, hipFuncAttributeMaxDynamicSharedMemorySize, A_LDS);
  (void)hipFuncSetAttribute((const void*)k3_out, hipFuncAttributeMaxDynamicSharedMemorySize, K3_LDS);
  k1_qkv_kgen<<<256, 512, K1_LDS, stream>>>(x, wqkv, u, lar, aim, cr, ci, ldt, (char*)d_ws);
  k2_attn<<<256, 1024, A_LDS, stream>>>((char*)d_ws);
  k3_out<<<512, 256, K3_LDS, stream>>>(wo, dsk, (const char*)d_ws, (u16*)d_out);
}
```

```cpp
#include <hip/hip_runtime.h>
#include <stdint.h>

#if defined(__HIP_DEVICE_COMPILE__)
#pragma clang attribute push(__attribute__((target("no-packed-fp32-ops"))), apply_to = function)
#define TIDX ((int)__builtin_amdgcn_workitem_id_x())
#define BIDX ((int)__builtin_amdgcn_workgroup_id_x())
#else
#define TIDX 0
#define BIDX 0
#endif
#define U2F(x) __builtin_bit_cast(float, (unsigned)(x))
#define F2U(x) __builtin_bit_cast(unsigned, (float)(x))

typedef unsigned short u16;
typedef __attribute__((ext_vector_type(8))) short bf16x8;
typedef __attribute__((ext_vector_type(4))) short s16x4;
typedef __attribute__((ext_vector_type(16))) float f32x16;
typedef __attribute__((ext_vector_type(4))) float f32x4;
typedef __attribute__((ext_vector_type(2))) float f32x2;
typedef __attribute__((ext_vector_type(4))) unsigned u32x4;
typedef __attribute__((ext_vector_type(2))) unsigned u32x2;
typedef __attribute__((ext_vector_type(2))) __bf16 bf2_t;
#define DI __device__ __forceinline__
#define LAS __attribute__((address_space(3)))
typedef LAS char* ldsp;

#define MFMA32(a, b, c) __builtin_amdgcn_mfma_f32_32x32x16_bf16((a), (b), (c), 0, 0, 0)
#define MFMA16(a, b, c) __builtin_amdgcn_mfma_f32_16x16x32_bf16((a), (b), (c), 0, 0, 0)
#define MFMAF(a, b, c) __builtin_amdgcn_mfma_f32_16x16x4f32((a), (b), (c), 0, 0, 0)
#define WAITBAR(N) asm volatile("s_waitcnt vmcnt(" #N ") lgkmcnt(0)\n\ts_barrier" ::: "memory")
#define WAITV(N) asm volatile("s_waitcnt vmcnt(" #N ")" ::: "memory")
#define LBAR() asm volatile("s_waitcnt lgkmcnt(0)\n\ts_barrier" ::: "memory")
#define PIN(x) asm volatile("" : "+v"(x))

DI float bf2f(unsigned x) { return U2F(x << 16); }
DI unsigned cvtpk(float a, float b) { f32x2 v = {a, b}; bf2_t r = __builtin_convertvector(v, bf2_t); return __builtin_bit_cast(unsigned, r); }
DI float rbf(float f) { return U2F(cvtpk(f, 0.f) << 16); }
DI float sel(bool c, float a, float b) { PIN(a); PIN(b); return c ? a : b; }
DI void glds16(const void* g, unsigned lds_base) {
  unsigned sv;
  asm volatile("s_mov_b32 %0, m0\n\ts_mov_b32 m0, %2\n\ts_nop 0\n\tglobal_load_lds_dwordx4 %1, off\n\ts_mov_b32 m0, %0"
               : "=&s"(sv) : "v"(g), "s"(lds_base) : "memory");
}
DI s16x4 trrd(ldsp p) { return __builtin_amdgcn_ds_read_tr16_b64_v4i16((LAS s16x4*)p); }
DI bf16x8 ldsrd(ldsp p) { return *(LAS bf16x8*)p; }
DI unsigned rfl(unsigned x) { return __builtin_amdgcn_readfirstlane(x); }

constexpr int SEQ = 2048, DM = 512, NST = 128, NH = 32;
constexpr size_t WS_Q = 0, WS_K = 2u << 20, WS_V = 4u << 20, WS_O = 6u << 20, WS_KR = 8u << 20, WS_UT = 12u << 20, WS_UT1 = 16u << 20;
constexpr int RP = 2560;
constexpr int RU = 2240;
constexpr int RUU = 2272;
constexpr int KX0 = 2079;
constexpr int U0 = 192;
constexpr float QSCALE = 0.25f * 1.4426950408889634f;

constexpr int K1_SLOT = 14336, K1_WOFF = 8192, K1_NS = 5;
constexpr int K1_PSZ = 3 * 256 * 4, K1_PART = 0, K1_PAR = 8 * K1_PSZ, K1_PAR2 = K1_PAR + 4096, K1_TR = K1_PAR2 + 1024;
constexpr int K1_LDS = K1_NS * K1_SLOT;

DI int kbase(int T, int i) {
  if (T == 0) return 128 * (i >> 3) + (i & 7);
  if (T == 1) return i < 8 ? 256 + 128 * (i >> 2) + 2 * (i & 3) : 512 + 128 * ((i - 8) >> 1) + 4 * (i & 1);
  return i < 8 ? 1024 + 128 * i : 2048;
}
DI void kgen_state(ldsp pp, ldsp pp2, const float (&ps)[4], float (&bv)[2], float (&av)[3][2]) {
  const f32x4 pr = *(LAS f32x4*)pp;
  const float cr = pr[3], ci = *(LAS float*)pp2;
  float e[4], f[4], c[4], s[4], k[4];
#pragma unroll
  for (int i = 0; i < 4; ++i) { k[i] = pr[1] * ps[i]; e[i] = pr[0] * ps[i]; }
#pragma unroll
  for (int i = 0; i < 4; ++i) { k[i] = __builtin_rintf(k[i]); e[i] = __builtin_amdgcn_exp2f(e[i]); }
#pragma unroll
  for (int i = 0; i < 4; ++i) f[i] = __builtin_fmaf(pr[1], ps[i], -k[i]);
#pragma unroll
  for (int i = 0; i < 4; ++i) f[i] = __builtin_fmaf(pr[2], ps[i], f[i]);
#pragma unroll
  for (int i = 0; i < 4; ++i) { c[i] = __builtin_amdgcn_cosf(f[i]); s[i] = __builtin_amdgcn_sinf(f[i]); }
  bv[0] = e[0] * c[0]; bv[1] = e[0] * s[0];
#pragma unroll
  for (int T = 0; T < 3; ++T) {
    av[T][0] = __builtin_fmaf(-ci, s[T + 1], cr * c[T + 1]) * e[T + 1];
    av[T][1] = __builtin_fmaf(ci, c[T + 1], cr * s[T + 1]) * -e[T + 1];
  }
}
DI void split2(float a, float b, unsigned& hi, unsigned& lo) {
  hi = cvtpk(a, b);
  lo = cvtpk(a - U2F(hi << 16), b - U2F(hi & 0xffff0000u));
}
DI void kgen_block(ldsp pp, ldsp pp2, const float (&ps)[4], f32x4 (&ka)[3]) {
  u32x4 bh, bl, ah[3], al[3];
#pragma unroll
  for (int q = 0; q < 4; ++q) {
    float bv[2], av[3][2];
    kgen_state(pp + q * 64, pp2 + q * 16, ps, bv, av);
    unsigned h, l;
    split2(bv[0], bv[1], h, l); bh[q] = h; bl[q] = l;
#pragma unroll
    for (int T = 0; T < 3; ++T) { split2(av[T][0], av[T][1], h, l); ah[T][q] = h; al[T][q] = l; }
  }
#define BC(x) __builtin_bit_cast(bf16x8, x)
#pragma unroll
  for (int T = 0; T < 3; ++T) ka[T] = MFMA16(BC(al[T]), BC(bh), ka[T]);
#pragma unroll
  for (int T = 0; T < 3; ++T) ka[T] = MFMA16(BC(ah[T]), BC(bl), ka[T]);
#pragma unroll
  for (int T = 0; T < 3; ++T) ka[T] = MFMA16(BC(ah[T]), BC(bh), ka[T]);
#undef BC
}

template <bool ISV>
DI void k1_gemm_loop(ldsp lds, unsigned lds0, int w, int rot, const u16* xsrc, const u16* wsrc, const unsigned (&xa)[2][2], unsigned wa,
                     f32x16 (&acc)[2]) {
#define K1_ISSUE(st)                                                                                                \
  do {                                                                                                              \
    const int ks_ = (((st) + rot) & 15) * 32;                                                                       \
    glds16(xsrc + ks_, rfl(lds0 + ((st) % K1_NS) * K1_SLOT + w * 1024));                                            \
    glds16(wsrc + (size_t)ks_ * 1536, rfl(lds0 + ((st) % K1_NS) * K1_SLOT + K1_WOFF + w * 1024));                   \
  } while (0)
#pragma unroll
  for (int st = 0; st < 16; ++st) {
    if (st < 13) WAITBAR(6); else if (st == 13) WAITBAR(4); else if (st == 14) WAITBAR(2); else WAITBAR(0);
    ldsp sl = lds + (st % K1_NS) * K1_SLOT;
    bf16x8 xf[2][2], wf[2];
#pragma unroll
    for (int i = 0; i < 2; ++i) {
      xf[0][i] = ldsrd(sl + xa[0][i]);
      xf[1][i] = ldsrd(sl + xa[1][i]);
      const s16x4 lo = trrd(sl + wa + i * 16 * 192), hi = trrd(sl + wa + i * 16 * 192 + 4 * 192);
      wf[i] = __builtin_shufflevector(lo, hi, 0, 1, 2, 3, 4, 5, 6, 7);
    }
    if (st >= 1 && st + K1_NS - 1 < 16) K1_ISSUE(st + K1_NS - 1);
#pragma unroll
    for (int i = 0; i < 2; ++i) {
      if (ISV) { acc[0] = MFMA32(xf[0][i], wf[i], acc[0]); acc[1] = MFMA32(xf[1][i], wf[i], acc[1]); }
      else     { acc[0] = MFMA32(wf[i], xf[0][i], acc[0]); acc[1] = MFMA32(wf[i], xf[1][i], acc[1]); }
    }
  }
#undef K1_ISSUE
}

DI void k1_gemm(ldsp lds, const u16* __restrict__ X, const u16* __restrict__ W, char* __restrict__ ws) {
  const unsigned lds0 = (unsigned)(uintptr_t)lds;
  const int tid = TIDX, lane = tid & 63, w = rfl(tid >> 6), bid = BIDX - 256;
  const int xcd = bid & 7, idx = bid >> 3;
  const int mt = (xcd >> 1) * 4 + (idx >> 3), nt = (xcd & 1) * 8 + (idx & 7);
  const int m0 = mt * 128, n0 = nt * 96;
  const int r = lane & 31, hh = lane >> 5;
  const int rot = 2 * ((idx + (idx >> 3)) & 7);
  const u16* xsrc;
  const u16* wsrc;
  {
    const int row = w * 16 + (lane >> 2);
    xsrc = X + (size_t)(m0 + row) * DM + (((lane & 3) ^ ((row >> 2) & 3)) << 3);
    const int off = (w < 6 ? w : 0) * 1024 + lane * 16, wr = off / 192, cch = (off - wr * 192) >> 4;
    wsrc = W + (size_t)wr * 1536 + n0 + cch * 8;
  }
  if (w >= 6) {
#pragma unroll
    for (int st = 0; st < K1_NS; ++st) glds16(xsrc + ((st + rot) & 15) * 32, rfl(lds0 + st * K1_SLOT + w * 1024));
#pragma unroll
    for (int st = 0; st < 16; ++st) {
      if (st < 13) WAITBAR(3); else if (st == 13) WAITBAR(2); else if (st == 14) WAITBAR(1); else WAITBAR(0);
      if (st >= 1 && st + K1_NS - 1 < 16)
        glds16(xsrc + ((st + K1_NS - 1 + rot) & 15) * 32, rfl(lds0 + ((st + K1_NS - 1) % K1_NS) * K1_SLOT + w * 1024));
    }
    return;
  }
#pragma unroll
  for (int st = 0; st < K1_NS; ++st) {
    const int ks = ((st + rot) & 15) * 32;
    glds16(xsrc + ks, rfl(lds0 + st * K1_SLOT + w * 1024));
    glds16(wsrc + (size_t)ks * 1536, rfl(lds0 + st * K1_SLOT + K1_WOFF + w * 1024));
  }
  const int mi = w & 1, nj = w >> 1;
  const int cg = n0 + 32 * nj;
  unsigned xa[2][2];
#pragma unroll
  for (int a = 0; a < 2; ++a) {
    const int row = 64 * mi + 32 * a + r;
#pragma unroll
    for (int i = 0; i < 2; ++i) xa[a][i] = row * 64 + (((2 * i + hh) ^ ((row >> 2) & 3)) << 4);
  }
  const unsigned wa = (unsigned)(K1_WOFF + (8 * hh + ((lane & 15) >> 2)) * 192 + (32 * nj + 16 * ((lane >> 4) & 1) + 4 * (lane & 3)) * 2);
  f32x16 acc[2] = {};
  const int hb = (cg & 511) >> 4;
  if (cg >= 1024) {
    k1_gemm_loop<true>(lds, lds0, w, rot, xsrc, wsrc, xa, wa, acc);
#pragma unroll
    for (int a = 0; a < 2; ++a) {
      const int mtile = mt * 4 + mi * 2 + a;
      const int h = hb + (r >> 4), d = r & 15, si = d < 4 ? d : (d < 8 ? d + 8 : d - 4);
      u16* dst = (u16*)(ws + WS_V) + ((((size_t)h * 128 + 2 * mtile) * 2 + hh) * 16 + si) * 8;
#pragma unroll
      for (int s = 0; s < 2; ++s) {
        u32x4 o = {cvtpk(acc[a][8 * s], acc[a][8 * s + 1]), cvtpk(acc[a][8 * s + 2], acc[a][8 * s + 3]),
                   cvtpk(acc[a][8 * s + 4], acc[a][8 * s + 5]), cvtpk(acc[a][8 * s + 6], acc[a][8 * s + 7])};
        __builtin_nontemporal_store(o, (u32x4*)(dst + s * 256));
      }
    }
  } else {
    k1_gemm_loop<false>(lds, lds0, w, rot, xsrc, wsrc, xa, wa, acc);
    const bool isq = cg < 512;
    const float sc = isq ? QSCALE : 1.f;
#pragma unroll
    for (int a = 0; a < 2; ++a) {
      const int mtile = mt * 4 + mi * 2 + a;
      u16* dst = (u16*)(ws + (isq ? WS_Q : WS_K)) + ((((size_t)hb * 64 + mtile) * 2 + hh) * 32 + r) * 8;
#pragma unroll
      for (int s = 0; s < 2; ++s) {
        u32x4 o = {cvtpk(acc[a][8 * s] * sc, acc[a][8 * s + 1] * sc), cvtpk(acc[a][8 * s + 2] * sc, acc[a][8 * s + 3] * sc),
                   cvtpk(acc[a][8 * s + 4] * sc, acc[a][8 * s + 5] * sc), cvtpk(acc[a][8 * s + 6] * sc, acc[a][8 * s + 7] * sc)};
        __builtin_nontemporal_store(o, (u32x4*)(dst + (size_t)s * 64 * 512));
      }
    }
  }
}

DI void k1_kgen(ldsp lds, const u16* __restrict__ U, const u16* __restrict__ LAR, const u16* __restrict__ AIM, const u16* __restrict__ CR,
                const u16* __restrict__ CI, const u16* __restrict__ LDT, char* __restrict__ ws) {
  const int tid = TIDX, lane = tid & 63, w = rfl(tid >> 6), bid = BIDX;
  const int r = lane & 31;
  const int ch = w >> 2, pch = 2 * bid + ch, pi = pch * NST + (w & 3) * 32 + r;
  const unsigned plar = LAR[pi], paim = AIM[pi], pcr = CR[pi], pci = CI[pi], pldt = LDT[pch];
  const int tl0 = (bid >> 3) * 64, td0 = (bid & 7) * 64 + w * 8;
  const u32x4 ua = *(const u32x4*)(U + (size_t)(tl0 + lane) * DM + td0);
  u32x4 uc = *(const u32x4*)(U + (size_t)(tl0 > 0 ? tl0 - 1 : 0) * DM + td0);
  {
    const float dt = rbf(__builtin_expf(bf2f(pldt)));
    const float ar = rbf(__builtin_expf(bf2f(plar)));
    const float zi = bf2f(paim) * dt, cr = bf2f(pcr) * dt, ci = bf2f(pci) * dt;
    const float HI = 0.15915493667125702f, LO = 6.4206382e-09f;
    const float wh = zi * HI;
    const float wl = __builtin_fmaf(zi, HI, -wh) + zi * LO;
    if (lane < 32) {
      *(LAS f32x4*)(lds + K1_PAR + (w * 32 + lane) * 16) = (f32x4){-(ar * dt) * 1.4426950408889634f, wh, wl, cr};
      *(LAS float*)(lds + K1_PAR2 + (w * 32 + lane) * 4) = ci;
    }
  }
  {
    ldsp tp = lds + K1_TR + w * 1056;
    *(LAS u32x4*)(tp + (lane + 1) * 16) = ua;
    if (lane == 0) {
      if (tl0 == 0) uc = (u32x4){0u, 0u, 0u, 0u};
      *(LAS u32x4*)(tp) = uc;
    }
    const int d = lane >> 3, ls = lane & 7;
    unsigned v[9];
#pragma unroll
    for (int i = 0; i < 9; ++i) v[i] = *(LAS u16*)(tp + (ls * 8 + i) * 16 + d * 2);
    const u32x4 t0 = {v[1] | (v[2] << 16), v[3] | (v[4] << 16), v[5] | (v[6] << 16), v[7] | (v[8] << 16)};
    const u32x4 t1 = {v[0] | (v[1] << 16), v[2] | (v[3] << 16), v[4] | (v[5] << 16), v[6] | (v[7] << 16)};
    const size_t o = (size_t)(td0 + d) * RP + U0 + tl0 + ls * 8;
    __builtin_nontemporal_store(t0, (u32x4*)((u16*)(ws + WS_UT) + o));
    __builtin_nontemporal_store(t1, (u32x4*)((u16*)(ws + WS_UT1) + o));
    if (tl0 == SEQ - 64 && ls == 7) *(u32x4*)((u16*)(ws + WS_UT1) + o + 8) = (u32x4){v[8], 0u, 0u, 0u};
  }
  if (tid < 112) {
    const int a = tid / 28, c = tid - a * 28;
    const int c8 = c < 24 ? c : (U0 + SEQ) / 8 + (c - 24);
    u16* dst = (u16*)(ws + ((a & 1) ? WS_UT1 : WS_UT)) + (size_t)(2 * bid + (a >> 1)) * RP + c8 * 8;
    if (!((a & 1) && c == 24)) *(u32x4*)dst = (u32x4){0u, 0u, 0u, 0u};
  }
  const int ki = lane & 15, kg = lane >> 4;
  const float ps[4] = {(float)(8 * ki), (float)kbase(0, ki), (float)kbase(1, ki), (float)kbase(2, ki)};
  f32x4 ka[3] = {};
  ldsp pp = lds + K1_PAR + (w * 32 + kg) * 16;
  ldsp pp2 = lds + K1_PAR2 + (w * 32 + kg) * 4;
  kgen_block(pp, pp2, ps, ka);
  kgen_block(pp + 256, pp2 + 64, ps, ka);
#pragma unroll
  for (int T = 0; T < 3; ++T) *(LAS f32x4*)(lds + K1_PART + w * K1_PSZ + (T * 64 + lane) * 16) = ka[T];
  LBAR();
  if ((w & 3) < 3) {
    const int T = w & 3;
    f32x4 v = *(LAS f32x4*)(lds + K1_PART + (4 * ch) * K1_PSZ + (T * 64 + lane) * 16);
#pragma unroll
    for (int q = 1; q < 4; ++q) v += *(LAS f32x4*)(lds + K1_PART + (4 * ch + q) * K1_PSZ + (T * 64 + lane) * 16);
    __builtin_nontemporal_store(v * 2.f, (f32x4*)(ws + WS_KR + ((size_t)pch * 3 + T) * 1024 + lane * 16));
  }
}

__global__ __launch_bounds__(512, 4) void k1_qkv_kgen(const u16* __restrict__ X, const u16* __restrict__ W, const u16* __restrict__ U,
                                                      const u16* __restrict__ LAR, const u16* __restrict__ AIM, const u16* __restrict__ CR,
                                                      const u16* __restrict__ CI, const u16* __restrict__ LDT, char* __restrict__ ws) {
  extern __shared__ __attribute__((aligned(16))) char smem[];
  if (BIDX >= 256) k1_gemm((ldsp)smem, X, W, ws);
  else k1_kgen((ldsp)smem, U, LAR, AIM, CR, CI, LDT, ws);
}

constexpr int A_K = 0, A_V = 65536, A_Q = 131072, A_ONE = 139264, A_X = 143360, A_LDS = 159744;

DI void attn_pv(const f32x16& s_in, const bf16x8& v0, const bf16x8& v1, f32x16& o) {
  f32x16 s;
#pragma unroll
  for (int i = 0; i < 8; ++i) s[i] = __builtin_amdgcn_exp2f(s_in[i]);
  const u32x4 p0 = {cvtpk(s[0], s[1]), cvtpk(s[2], s[3]), cvtpk(s[4], s[5]), cvtpk(s[6], s[7])};
  o = MFMA32(v0, __builtin_bit_cast(bf16x8, p0), o);
#pragma unroll
  for (int i = 8; i < 16; ++i) s[i] = __builtin_amdgcn_exp2f(s_in[i]);
  const u32x4 p1 = {cvtpk(s[8], s[9]), cvtpk(s[10], s[11]), cvtpk(s[12], s[13]), cvtpk(s[14], s[15])};
  o = MFMA32(v1, __builtin_bit_cast(bf16x8, p1), o);
}
DI void attn_run(ldsp& kp, ldsp& vp, unsigned vstep, int n, const bf16x8& qf, const f32x16& negm, f32x16& o) {
  f32x16 sa = MFMA32(ldsrd(kp), qf, negm);
#pragma unroll 1
  for (int it = 0; it < n; ++it) {
    const bf16x8 k1 = ldsrd(kp + 1024);
    const bf16x8 va0 = ldsrd(vp), va1 = ldsrd(vp + 512);
    const f32x16 sb = MFMA32(k1, qf, negm);
    const bf16x8 k2 = ldsrd(kp + 2048);
    const bf16x8 vb0 = ldsrd(vp + 1024), vb1 = ldsrd(vp + 1536);
    attn_pv(sa, va0, va1, o);
    sa = MFMA32(k2, qf, negm);
    attn_pv(sb, vb0, vb1, o);
    kp += 2048; vp += vstep;
  }
}
DI float tile_max(const f32x16& s) {
  float m = __builtin_fmaxf(__builtin_fmaxf(s[0], s[1]), __builtin_fmaxf(s[2], s[3]));
#pragma unroll
  for (int i = 4; i < 16; i += 4) m = __builtin_fmaxf(m, __builtin_fmaxf(__builtin_fmaxf(s[i], s[i + 1]), __builtin_fmaxf(s[i + 2], s[i + 3])));
  const auto sw = __builtin_amdgcn_permlane32_swap(F2U(m), F2U(m), false, false);
  return __builtin_fmaxf(U2F(sw[0]), U2F(sw[1]));
}

__global__ __launch_bounds__(1024, 4) void k2_attn(char* __restrict__ ws) {
  extern __shared__ __attribute__((aligned(16))) char smem[];
  ldsp lds = (ldsp)smem;
  const unsigned lds0 = (unsigned)(uintptr_t)lds;
  const int tid = TIDX, lane = tid & 63, w = rfl(tid >> 6), bid = BIDX;
  const int h = (bid & 7) * 4 + ((bid >> 3) & 3), qb = bid >> 5;
  const int qt = w & 7, kh = w >> 3, r = lane & 31, hh = lane >> 5;
  const char* kg = ws + WS_K + (size_t)h * 65536 + lane * 16;
  const char* vg = ws + WS_V + (size_t)h * 65536 + lane * 16;
  {
    const int pc = ((w >> 1) & 1) * 32 + (w & 1);
    const char* src = w < 8 ? ws + WS_Q + ((size_t)h * 64 + qb * 8 + w) * 1024 + lane * 16 : (w < 12 ? kg : vg) + pc * 1024;
    const unsigned dst = w < 8 ? A_Q + w * 1024 : (w < 12 ? A_K : A_V) + pc * 1024;
    glds16(src, rfl(lds0 + dst));
  }
  if (tid < 4) *(LAS u32x4*)(lds + A_ONE + tid * 512) = (u32x4){0x3f803f80u, 0x3f803f80u, 0x3f803f80u, 0x3f803f80u};
  const int di = r < 4 ? r : (r >= 12 && r < 16 ? r - 8 : (r >= 20 && r < 28 ? r - 12 : -1));
  ldsp kp = lds + A_K + kh * 32768 + lane * 16;
  ldsp vp = di >= 0 ? lds + A_V + kh * 32768 + hh * 256 + di * 16 : lds + A_ONE;
  const unsigned vstep = di >= 0 ? 2048u : 0u;
  const char* lsrc = ((w & 2) ? vg : kg) + (w & 1) * 32768 + (2 + (w >> 2)) * 1024;
  const unsigned ldst = lds0 + ((w & 2) ? A_V : A_K) + (w & 1) * 32768 + (2 + (w >> 2)) * 1024;
  WAITBAR(0);
  glds16(lsrc, rfl(ldst));
  glds16(lsrc + 4096, rfl(ldst + 4096));
  const bf16x8 qf = ldsrd(lds + A_Q + qt * 1024 + lane * 16);
  float m;
  {
    const f32x16 z = {};
    const f32x16 s = MFMA32(ldsrd(kp), qf, z);
    m = tile_max(s);
  }
  f32x16 negm, o = {};
#pragma unroll
  for (int i = 0; i < 16; ++i) negm[i] = -m;
  attn_run(kp, vp, vstep, 1, qf, negm, o);
  WAITBAR(0);
#pragma unroll
  for (int i = 2; i < 5; ++i) glds16(lsrc + i * 4096, rfl(ldst + i * 4096));
  attn_run(kp, vp, vstep, 2, qf, negm, o);
#pragma unroll
  for (int i = 5; i < 7; ++i) glds16(lsrc + i * 4096, rfl(ldst + i * 4096));
  if (w < 8) glds16(lsrc + 7 * 4096, rfl(ldst + 7 * 4096));
  attn_run(kp, vp, vstep, 2, qf, negm, o);
  WAITBAR(0);
  attn_run(kp, vp, vstep, 11, qf, negm, o);
  float l = sel(hh, o[0], o[4]);
  if (__builtin_expect(__builtin_amdgcn_ballot_w64(!(l <= 1.8446744e19f)) != 0, 0)) {
    kp = lds + A_K + kh * 32768 + lane * 16;
    vp = di >= 0 ? lds + A_V + kh * 32768 + hh * 256 + di * 16 : lds + A_ONE;
    const unsigned vs1 = di >= 0 ? 1024u : 0u;
    o = (f32x16){};
    const f32x16 z = {};
#pragma unroll 1
    for (int t = 0; t < 32; ++t) {
      f32x16 s = MFMA32(ldsrd(kp), qf, z);
      const bf16x8 v0 = ldsrd(vp), v1 = ldsrd(vp + 512);
      const float mn = __builtin_fmaxf(m, tile_max(s));
      const float al = __builtin_amdgcn_exp2f(m - mn);
      m = mn;
#pragma unroll
      for (int i = 0; i < 16; ++i) { o[i] *= al; s[i] -= mn; }
      attn_pv(s, v0, v1, o);
      kp += 1024; vp += vs1;
    }
    l = sel(hh, o[0], o[4]);
  }
  float od[8];
#pragma unroll
  for (int i = 0; i < 4; ++i) { od[i] = sel(hh, o[4 + i], o[i]); od[4 + i] = sel(hh, o[8 + i], o[12 + i]); }
  ldsp sp = lds + A_Q + (qt * 64 + lane) * 16;
  ldsp sq = lds + A_X + (qt * 2 * 64 + lane) * 16;
  if (kh == 1) {
    *(LAS f32x4*)(sp) = (f32x4){od[0], od[1], od[2], od[3]};
    *(LAS f32x4*)(sq) = (f32x4){od[4], od[5], od[6], od[7]};
    *(LAS f32x2*)(sq + 1024) = (f32x2){m, l};
  }
  LBAR();
  if (kh == 0) {
    const f32x4 a = *(LAS f32x4*)(sp), b = *(LAS f32x4*)(sq);
    const f32x2 ml = *(LAS f32x2*)(sq + 1024);
    const float mm = __builtin_fmaxf(m, ml[0]);
    const float s0 = __builtin_amdgcn_exp2f(m - mm), s1 = __builtin_amdgcn_exp2f(ml[0] - mm);
    const float inv = 1.f / (l * s0 + ml[1] * s1);
    const float f0 = s0 * inv, f1 = s1 * inv;
    float q[8];
#pragma unroll
    for (int i = 0; i < 4; ++i) { q[i] = od[i] * f0 + a[i] * f1; q[4 + i] = od[4 + i] * f0 + b[i] * f1; }
    const u32x4 ov = {cvtpk(q[0], q[1]), cvtpk(q[2], q[3]), cvtpk(q[4], q[5]), cvtpk(q[6], q[7])};
    __builtin_nontemporal_store(ov, (u32x4*)((u16*)(ws + WS_O) + (size_t)(qb * 256 + qt * 32 + r) * DM + h * 16 + hh * 8));
  }
}

constexpr int K3_LDS = 65536;
constexpr int CP = 5120 + 64, CCH = 3 * CP, CTAB = 2 * CCH;

DI void k3_oproj(ldsp lds, const u16* __restrict__ Wo, const char* __restrict__ ws, u16* __restrict__ out) {
  const int tid = TIDX, lane = tid & 63, w = rfl(tid >> 6), bid = BIDX - 256;
  const unsigned lds0 = (unsigned)(uintptr_t)lds;
  const int xcd = bid & 7, idx = bid >> 3;
  const int mt = xcd * 4 + (idx >> 3), nt = idx & 7, m0 = mt * 64, n0 = nt * 64;
  const int r = lane & 31, hh = lane >> 5, wm = w & 1, wn = (w >> 1) & 1, kk = w >> 2;
  const u16* O = (const u16*)(ws + WS_O);
  const int lrow = w * 8 + (lane >> 3), sl = lane & 7;
  const u16* as = O + (size_t)(m0 + lrow) * DM + ((sl ^ ((lrow >> 1) & 7)) << 3);
  const u16* bs = Wo + (size_t)lrow * DM + n0 + ((sl ^ (((lrow >> 1) & 1) << 2)) << 3);
  const int rot = (idx + (idx >> 3)) & 7;
#define K3_ISSUE(st)                                                                     \
  do {                                                                                   \
    const int ks_ = (((st) + rot) & 7) * 64;                                             \
    glds16(as + ks_, rfl(lds0 + ((st) & 3) * 16384 + w * 1024));                         \
    glds16(bs + (size_t)ks_ * DM, rfl(lds0 + ((st) & 3) * 16384 + 8192 + w * 1024));     \
  } while (0)
  K3_ISSUE(0); K3_ISSUE(1); K3_ISSUE(2); K3_ISSUE(3);
  unsigned xa[2], wa;
  {
    const int row = 32 * wm + r;
#pragma unroll
    for (int i = 0; i < 2; ++i) xa[i] = row * 128 + (((4 * kk + 2 * i + hh) ^ ((row >> 1) & 7)) << 4);
    const int q = (lane & 15) >> 2, p = lane & 3, blk = (lane >> 4) & 1;
    const unsigned cb = (unsigned)(64 * wn + 32 * blk + 8 * p) ^ (unsigned)(((q >> 1) & 1) << 6);
    wa = 8192 + (32 * kk + 8 * hh + q) * 128 + cb;
  }
  f32x16 acc = {};
#define K3_COMP(st)                                                                              \
  do {                                                                                           \
    bf16x8 xf[2], wf[2];                                                                         \
    _Pragma("unroll") for (int i = 0; i < 2; ++i) {                                              \
      xf[i] = ldsrd(lds + xa[i] + ((st) & 3) * 16384);                                           \
      const s16x4 lo = trrd(lds + wa + ((st) & 3) * 16384 + i * 2048);                           \
      const s16x4 hi = trrd(lds + wa + ((st) & 3) * 16384 + i * 2048 + 512);                     \
      wf[i] = __builtin_shufflevector(lo, hi, 0, 1, 2, 3, 4, 5, 6, 7);                           \
    }                                                                                            \
    acc = MFMA32(wf[0], xf[0], acc);                                                             \
    acc = MFMA32(wf[1], xf[1], acc);                                                             \
  } while (0)
  WAITBAR(6); K3_COMP(0);
  WAITBAR(4); K3_ISSUE(4); K3_COMP(1);
  WAITBAR(4); K3_ISSUE(5); K3_COMP(2);
  WAITBAR(4); K3_ISSUE(6); K3_COMP(3);
  WAITBAR(4); K3_ISSUE(7); K3_COMP(4);
  WAITBAR(4); K3_COMP(5);
  WAITBAR(2); K3_COMP(6);
  WAITBAR(0); K3_COMP(7);
  if (kk == 1) {
#pragma unroll
    for (int q = 0; q < 4; ++q)
      *(LAS f32x4*)(lds + (((w & 3) * 4 + q) * 64 + lane) * 16) = (f32x4){acc[4 * q], acc[4 * q + 1], acc[4 * q + 2], acc[4 * q + 3]};
  }
  LBAR();
  if (kk == 0) {
    u16* dst = out + (size_t)(m0 + 32 * wm + r) * 1024 + 512 + n0 + 32 * wn + 4 * hh;
#pragma unroll
    for (int q = 0; q < 4; ++q) {
      const f32x4 t = *(LAS f32x4*)(lds + ((w * 4 + q) * 64 + lane) * 16);
      const u32x2 ov = {cvtpk(acc[4 * q] + t[0], acc[4 * q + 1] + t[1]), cvtpk(acc[4 * q + 2] + t[2], acc[4 * q + 3] + t[3])};
      *(u32x2*)(dst + 8 * q) = ov;
    }
  }
#undef K3_ISSUE
#undef K3_COMP
}

DI bf16x8 rd4(ldsp p) {
  const u32x4 v = {*(LAS unsigned*)p, *(LAS unsigned*)(p + 4), *(LAS unsigned*)(p + 8), *(LAS unsigned*)(p + 12)};
  return __builtin_bit_cast(bf16x8, v);
}
DI void conv_class(ldsp kr, ldsp ut, int rho, f32x4 (&acc)[8]) {
  kr -= 64 * rho; ut -= 64 * rho;
  bf16x8 F[8];
#pragma unroll
  for (int t = 0; t < 8; ++t) F[t] = rd4(ut + 512 * (t + 1));
  if (rho == 0) {
    const bf16x8 fm = rd4(ut);
#pragma unroll
    for (int t = 0; t < 8; ++t) acc[t] = MFMA16(ldsrd(kr - 512 * (t + 1)), fm, acc[t]);
  }
#pragma unroll
  for (int s = 0; s < 8; ++s) {
    const bf16x8 g = ldsrd(kr - 512 * s);
#pragma unroll
    for (int t = s; t < 8; ++t) acc[t] = MFMA16(g, F[t - s], acc[t]);
  }
}

DI void k3_conv(ldsp lds, const u16* __restrict__ DSK, const char* __restrict__ ws, u16* __restrict__ out) {
  const int tid = TIDX, lane = tid & 63, w = rfl(tid >> 6), j = BIDX;
  const unsigned lds0 = (unsigned)(uintptr_t)lds;
  const int d0 = 2 * ((j & 7) * 32 + (j >> 3));
  if (w < 6) glds16(ws + WS_KR + ((size_t)d0 * 3 + w) * 1024 + lane * 16, rfl(lds0 + CTAB + w * 1024));
#pragma unroll
  for (int i = 0; i < 3; ++i) {
    const int p = w + 8 * i;
    if (p < 20) {
      const int row = p / 5, part = p - row * 5, ch = row >> 1, kind = row & 1;
      glds16(ws + (kind ? WS_UT1 : WS_UT) + ((size_t)(d0 + ch) * RP + part * 512 + lane * 8) * 2, rfl(lds0 + (3 * ch + 1 + kind) * CP + part * 1024));
    }
  }
  const int ch = w >> 2, wq = w & 3;
  const int n = lane & 15, g = lane >> 4;
  const int c = n & 7;
  ldsp kr = lds + ch * CCH + (KX0 + 97 - 8 * n + 8 * g) * 2;
  ldsp ut = lds + ch * CCH + CP * (2 - (c & 1)) + (U0 + 97 - 256 + 128 * (n >> 3) + c + (1 - (c & 1)) + 8 * g) * 2;
  const float ds0 = bf2f(DSK[d0]), ds1 = bf2f(DSK[d0 + 1]);
  unsigned tix[8];
  {
    const int t8 = tid & 255;
#pragma unroll
    for (int j = 0; j < 8; ++j) {
      const int lag = 2047 - (t8 * 8 + j);
      const int pos = (int)rbf((float)lag);
      const int a = pos >> 7, e = pos & 7, col = (pos >> 3) & 15;
      const int row = a < 2 ? 8 * a + e : (a < 4 ? 8 + 4 * a + (e >> 1) : (a < 8 ? 16 + 2 * a + (e >> 2) : 24 + a));
      tix[j] = CTAB + ch * 3072 + (row >> 4) * 1024 + (((row >> 2) & 3) * 16 + col) * 16 + (row & 3) * 4;
    }
    if (t8 < 24) *(LAS u32x4*)(lds + ch * CCH + (t8 < 4 ? t8 : 256 + t8) * 16) = (u32x4){0u, 0u, 0u, 0u};
  }
  if (w < 4) WAITBAR(3); else WAITBAR(2);
  {
    float v[8];
#pragma unroll
    for (int j = 0; j < 8; ++j) v[j] = *(LAS float*)(lds + tix[j]);
    *(LAS u32x4*)(lds + ch * CCH + (4 + (tid & 255)) * 16) = (u32x4){cvtpk(v[0], v[1]), cvtpk(v[2], v[3]), cvtpk(v[4], v[5]), cvtpk(v[6], v[7])};
  }
  WAITBAR(0);
  f32x4 acc[8] = {};
  conv_class(kr, ut, wq, acc);
  conv_class(kr, ut, wq + 4, acc);
  const int lo0 = 256 * w + 128 * (n >> 3) + 32 * g + c;
  float us0[4], us1[4];
#pragma unroll
  for (int e = 0; e < 4; ++e) {
    us0[e] = bf2f(*(LAS u16*)(lds + CP + (U0 + lo0 + 8 * e) * 2));
    us1[e] = bf2f(*(LAS u16*)(lds + CCH + CP + (U0 + lo0 + 8 * e) * 2));
  }
  LBAR();
#pragma unroll
  for (int t = 0; t < 8; ++t) *(LAS f32x4*)(lds + ((w * 8 + t) * 64 + lane) * 16) = acc[t];
  LBAR();
  f32x4 y0 = *(LAS f32x4*)(lds + (w * 64 + lane) * 16), y1 = *(LAS f32x4*)(lds + ((32 + w) * 64 + lane) * 16);
#pragma unroll
  for (int q = 1; q < 4; ++q) {
    y0 += *(LAS f32x4*)(lds + ((q * 8 + w) * 64 + lane) * 16);
    y1 += *(LAS f32x4*)(lds + (((q + 4) * 8 + w) * 64 + lane) * 16);
  }
#pragma unroll
  for (int e = 0; e < 4; ++e)
    *(unsigned*)(out + (size_t)(lo0 + 8 * e) * 1024 + d0) = cvtpk(rbf(y0[e]) + rbf(us0[e] * ds0), rbf(y1[e]) + rbf(us1[e] * ds1));
}

__global__ __launch_bounds__(512, 4) void k3_out(const u16* __restrict__ Wo, const u16* __restrict__ DSK, const char* __restrict__ ws,
                                                 u16* __restrict__ out) {
  extern __shared__ __attribute__((aligned(16))) char smem[];
  if (BIDX >= 256) k3_oproj((ldsp)smem, Wo, ws, out);
  else k3_conv((ldsp)smem, DSK, ws, out);
}

#if defined(__HIP_DEVICE_COMPILE__)
#pragma clang attribute pop
#endif

extern "C" void kernel_launch(void* const* d_in, const int* in_sizes, int n_in, void* d_out, int out_size, void* d_ws, size_t ws_size,
                              hipStream_t stream) {
  const u16* u = (const u16*)d_in[0];
  const u16* lar = (const u16*)d_in[1];
  const u16* aim = (const u16*)d_in[2];
  const u16* cr = (const u16*)d_in[3];
  const u16* ci = (const u16*)d_in[4];
  const u16* ldt = (const u16*)d_in[5];
  const u16* dsk = (const u16*)d_in[6];
  const u16* x = (const u16*)d_in[7];
  const u16* wqkv = (const u16*)d_in[8];
  const u16* wo = (const u16*)d_in[9];
  (void)hipFuncSetAttribute((const void*)k1_qkv_kgen, hipFuncAttributeMaxDynamicSharedMemorySize, K1_LDS);
  (void)hipFuncSetAttribute((const void*)k2_attn, hipFuncAttributeMaxDynamicSharedMemorySize, A_LDS);
  (void)hipFuncSetAttribute((const void*)k3_out, hipFuncAttributeMaxDynamicSharedMemorySize, K3_LDS);
  k1_qkv_kgen<<<512, 512, K1_LDS, stream>>>(x, wqkv, u, lar, aim, cr, ci, ldt, (char*)d_ws);
  k2_attn<<<256, 1024, A_LDS, stream>>>((char*)d_ws);
  k3_out<<<512, 512, K3_LDS, stream>>>(wo, dsk, (const char*)d_ws, (u16*)d_out);
}
```

```cpp
#include <hip/hip_runtime.h>
#include <stdint.h>

#if defined(__HIP_DEVICE_COMPILE__)
#pragma clang attribute push(__attribute__((target("no-packed-fp32-ops"))), apply_to = function)
#define TIDX ((int)__builtin_amdgcn_workitem_id_x())
#define BIDX ((int)__builtin_amdgcn_workgroup_id_x())
#else
#define TIDX 0
#define BIDX 0
#endif
#define U2F(x) __builtin_bit_cast(float, (unsigned)(x))
#define F2U(x) __builtin_bit_cast(unsigned, (float)(x))

typedef unsigned short u16;
typedef __attribute__((ext_vector_type(8))) short bf16x8;
typedef __attribute__((ext_vector_type(4))) short s16x4;
typedef __attribute__((ext_vector_type(16))) float f32x16;
typedef __attribute__((ext_vector_type(4))) float f32x4;
typedef __attribute__((ext_vector_type(2))) float f32x2;
typedef __attribute__((ext_vector_type(4))) unsigned u32x4;
typedef __attribute__((ext_vector_type(2))) unsigned u32x2;
typedef __attribute__((ext_vector_type(2))) __bf16 bf2_t;
#define DI __device__ __forceinline__
#define LAS __attribute__((address_space(3)))
typedef LAS char* ldsp;

#define MFMA32(a, b, c) __builtin_amdgcn_mfma_f32_32x32x16_bf16((a), (b), (c), 0, 0, 0)
#define MFMA16(a, b, c) __builtin_amdgcn_mfma_f32_16x16x32_bf16((a), (b), (c), 0, 0, 0)
#define MFMAF(a, b, c) __builtin_amdgcn_mfma_f32_16x16x4f32((a), (b), (c), 0, 0, 0)
#define WAITBAR(N) asm volatile("s_waitcnt vmcnt(" #N ") lgkmcnt(0)\n\ts_barrier" ::: "memory")
#define WAITV(N) asm volatile("s_waitcnt vmcnt(" #N ")" ::: "memory")
#define LBAR() asm volatile("s_waitcnt lgkmcnt(0)\n\ts_barrier" ::: "memory")
#define PIN(x) asm volatile("" : "+v"(x))

DI float bf2f(unsigned x) { return U2F(x << 16); }
DI unsigned cvtpk(float a, float b) { f32x2 v = {a, b}; bf2_t r = __builtin_convertvector(v, bf2_t); return __builtin_bit_cast(unsigned, r); }
DI float rbf(float f) { return U2F(cvtpk(f, 0.f) << 16); }
DI float sel(bool c, float a, float b) { PIN(a); PIN(b); return c ? a : b; }
DI void glds16(const void* g, unsigned lds_base) {
  unsigned sv;
  asm volatile("s_mov_b32 %0, m0\n\ts_mov_b32 m0, %2\n\ts_nop 0\n\tglobal_load_lds_dwordx4 %1, off\n\ts_mov_b32 m0, %0"
               : "=&s"(sv) : "v"(g), "s"(lds_base) : "memory");
}
DI s16x4 trrd(ldsp p) { return __builtin_amdgcn_ds_read_tr16_b64_v4i16((LAS s16x4*)p); }
DI bf16x8 ldsrd(ldsp p) { return *(LAS bf16x8*)p; }
DI unsigned rfl(unsigned x) { return __builtin_amdgcn_readfirstlane(x); }

constexpr int SEQ = 2048, DM = 512, NST = 128, NH = 32;
constexpr size_t WS_Q = 0, WS_K = 2u << 20, WS_V = 4u << 20, WS_O = 6u << 20, WS_KR = 8u << 20, WS_UT = 12u << 20, WS_UT1 = 16u << 20;
constexpr int RP = 2560;
constexpr int RU = 2240;
constexpr int RUU = 2272;
constexpr int KX0 = 2079;
constexpr int U0 = 192;
constexpr float QSCALE = 0.25f * 1.4426950408889634f;

constexpr int K1_SLOT = 16384, K1_WOFF = 4096, K1_NS = 5;
constexpr int K1_PSZ = 3 * 256 * 4, K1_PART = 0, K1_PAR = 8 * K1_PSZ, K1_PAR2 = K1_PAR + 4096, K1_TR = K1_PAR2 + 1024, K1_DUMP = K1_TR + 8 * 1056, K1_TAB = K1_DUMP + 1024;
constexpr int K1_LDS = K1_NS * K1_SLOT;

DI int kbase(int T, int i) {
  if (T == 0) return 128 * (i >> 3) + (i & 7);
  if (T == 1) return i < 8 ? 256 + 128 * (i >> 2) + 2 * (i & 3) : 512 + 128 * ((i - 8) >> 1) + 4 * (i & 1);
  return i < 8 ? 1024 + 128 * i : 2048;
}
DI void kgen_state(ldsp pp, ldsp pp2, const float (&ps)[4], float (&bv)[2], float (&av)[3][2]) {
  const f32x4 pr = *(LAS f32x4*)pp;
  const float cr = pr[3], ci = *(LAS float*)pp2;
  float e[4], f[4], c[4], s[4], k[4];
#pragma unroll
  for (int i = 0; i < 4; ++i) { k[i] = pr[1] * ps[i]; e[i] = pr[0] * ps[i]; }
#pragma unroll
  for (int i = 0; i < 4; ++i) { k[i] = __builtin_rintf(k[i]); e[i] = __builtin_amdgcn_exp2f(e[i]); }
#pragma unroll
  for (int i = 0; i < 4; ++i) f[i] = __builtin_fmaf(pr[1], ps[i], -k[i]);
#pragma unroll
  for (int i = 0; i < 4; ++i) f[i] = __builtin_fmaf(pr[2], ps[i], f[i]);
#pragma unroll
  for (int i = 0; i < 4; ++i) { c[i] = __builtin_amdgcn_cosf(f[i]); s[i] = __builtin_amdgcn_sinf(f[i]); }
  bv[0] = e[0] * c[0]; bv[1] = e[0] * s[0];
#pragma unroll
  for (int T = 0; T < 3; ++T) {
    av[T][0] = __builtin_fmaf(-ci, s[T + 1], cr * c[T + 1]) * e[T + 1];
    av[T][1] = __builtin_fmaf(ci, c[T + 1], cr * s[T + 1]) * -e[T + 1];
  }
}
DI void split2(float a, float b, unsigned& hi, unsigned& lo) {
  hi = cvtpk(a, b);
  lo = cvtpk(a - U2F(hi << 16), b - U2F(hi & 0xffff0000u));
}
DI void kgen_block(ldsp pp, ldsp pp2, const float (&ps)[4], f32x4 (&ka)[3]) {
  u32x4 bh, bl, ah[3], al[3];
#pragma unroll
  for (int q = 0; q < 4; ++q) {
    float bv[2], av[3][2];
    kgen_state(pp + q * 64, pp2 + q * 16, ps, bv, av);
    unsigned h, l;
    split2(bv[0], bv[1], h, l); bh[q] = h; bl[q] = l;
#pragma unroll
    for (int T = 0; T < 3; ++T) { split2(av[T][0], av[T][1], h, l); ah[T][q] = h; al[T][q] = l; }
  }
#define BC(x) __builtin_bit_cast(bf16x8, x)
#pragma unroll
  for (int T = 0; T < 3; ++T) ka[T] = MFMA16(BC(al[T]), BC(bh), ka[T]);
#pragma unroll
  for (int T = 0; T < 3; ++T) ka[T] = MFMA16(BC(ah[T]), BC(bl), ka[T]);
#pragma unroll
  for (int T = 0; T < 3; ++T) ka[T] = MFMA16(BC(ah[T]), BC(bh), ka[T]);
#undef BC
}

template <bool ISV>
DI void k1_gemm_loop(ldsp lds, unsigned lds0, int w, int rot, const u16* src0, const u16* src1, size_t step0, const unsigned (&xa)[2][2],
                     unsigned wa, f32x16 (&acc)[2]) {
#pragma unroll
  for (int st = 0; st < 16; ++st) {
    if (st < 13) WAITBAR(6); else if (st == 13) WAITBAR(4); else if (st == 14) WAITBAR(2); else WAITBAR(0);
    ldsp sl = lds + (st % K1_NS) * K1_SLOT;
    bf16x8 xf[2][2], wf[2];
#pragma unroll
    for (int i = 0; i < 2; ++i) {
      xf[0][i] = ldsrd(sl + xa[0][i]);
      xf[1][i] = ldsrd(sl + xa[1][i]);
      const s16x4 lo = trrd(sl + wa + i * 16 * 384), hi = trrd(sl + wa + i * 16 * 384 + 4 * 384);
      wf[i] = __builtin_shufflevector(lo, hi, 0, 1, 2, 3, 4, 5, 6, 7);
    }
    if (st >= 1 && st + K1_NS - 1 < 16) {
      const int ks = (st + K1_NS - 1 + rot) & 15;
      glds16(src0 + ks * step0, rfl(lds0 + ((st + K1_NS - 1) % K1_NS) * K1_SLOT + w * 1024));
      glds16(src1 + (size_t)ks * (32 * 1536), rfl(lds0 + ((st + K1_NS - 1) % K1_NS) * K1_SLOT + (8 + w) * 1024));
    }
#pragma unroll
    for (int i = 0; i < 2; ++i) {
      if (ISV) { acc[0] = MFMA32(xf[0][i], wf[i], acc[0]); acc[1] = MFMA32(xf[1][i], wf[i], acc[1]); }
      else     { acc[0] = MFMA32(wf[i], xf[0][i], acc[0]); acc[1] = MFMA32(wf[i], xf[1][i], acc[1]); }
    }
  }
}
DI const u16* k1_wsrc(const u16* W, int p, int lane, int xcd) {
  const int off = p * 1024 + lane * 16, row = off / 384, ch = (off - row * 384) >> 4;
  const int cs = ch ^ (((row >> 1) & 1) << 2);
  return W + (size_t)row * 1536 + (cs >> 3) * 512 + xcd * 64 + (cs & 7) * 8;
}

DI void k1_gemm(ldsp lds, const u16* __restrict__ X, const u16* __restrict__ W, char* __restrict__ ws) {
  const unsigned lds0 = (unsigned)(uintptr_t)lds;
  const int tid = TIDX, lane = tid & 63, w = rfl(tid >> 6), bid = BIDX - 256;
  const int xcd = bid & 7, mt = bid >> 3;
  const int m0 = mt * 64;
  const int r = lane & 31, hh = lane >> 5;
  const int rot = 2 * (mt & 7);
  const u16* src0;
  size_t step0;
  if (w < 4) {
    const int row = w * 16 + (lane >> 2);
    src0 = X + (size_t)(m0 + row) * DM + (((lane & 3) ^ ((row >> 2) & 3)) << 3);
    step0 = 32;
  } else {
    src0 = k1_wsrc(W, w - 4, lane, xcd);
    step0 = 32 * 1536;
  }
  const u16* src1 = k1_wsrc(W, w + 4, lane, xcd);
#pragma unroll
  for (int st = 0; st < K1_NS; ++st) {
    const int ks = (st + rot) & 15;
    glds16(src0 + ks * step0, rfl(lds0 + st * K1_SLOT + w * 1024));
    glds16(src1 + (size_t)ks * (32 * 1536), rfl(lds0 + st * K1_SLOT + (8 + w) * 1024));
  }
  if (w >= 6) {
#pragma unroll
    for (int st = 0; st < 16; ++st) {
      if (st < 13) WAITBAR(6); else if (st == 13) WAITBAR(4); else if (st == 14) WAITBAR(2); else WAITBAR(0);
      if (st >= 1 && st + K1_NS - 1 < 16) {
        const int ks = (st + K1_NS - 1 + rot) & 15;
        glds16(src0 + ks * step0, rfl(lds0 + ((st + K1_NS - 1) % K1_NS) * K1_SLOT + w * 1024));
        glds16(src1 + (size_t)ks * (32 * 1536), rfl(lds0 + ((st + K1_NS - 1) % K1_NS) * K1_SLOT + (8 + w) * 1024));
      }
    }
    return;
  }
  unsigned xa[2][2];
#pragma unroll
  for (int a = 0; a < 2; ++a) {
    const int row = 32 * a + r;
#pragma unroll
    for (int i = 0; i < 2; ++i) xa[a][i] = row * 64 + (((2 * i + hh) ^ ((row >> 2) & 3)) << 4);
  }
  const int q = (lane & 15) >> 2;
  const unsigned wa = (unsigned)(K1_WOFF + (8 * hh + q) * 384 + ((64 * w) ^ (((q >> 1) & 1) << 6)) + 32 * ((lane >> 4) & 1) + 8 * (lane & 3));
  f32x16 acc[2] = {};
  const int hb = xcd * 4 + (w & 1) * 2;
  if (w >= 4) {
    k1_gemm_loop<true>(lds, lds0, w, rot, src0, src1, step0, xa, wa, acc);
#pragma unroll
    for (int a = 0; a < 2; ++a) {
      const int mtile = mt * 2 + a;
      const int h = hb + (r >> 4), d = r & 15, si = d < 4 ? d : (d < 8 ? d + 8 : d - 4);
      u16* dst = (u16*)(ws + WS_V) + ((((size_t)h * 128 + 2 * mtile) * 2 + hh) * 16 + si) * 8;
#pragma unroll
      for (int s = 0; s < 2; ++s) {
        u32x4 o = {cvtpk(acc[a][8 * s], acc[a][8 * s + 1]), cvtpk(acc[a][8 * s + 2], acc[a][8 * s + 3]),
                   cvtpk(acc[a][8 * s + 4], acc[a][8 * s + 5]), cvtpk(acc[a][8 * s + 6], acc[a][8 * s + 7])};
        *(u32x4*)(dst + s * 256) = o;
      }
    }
  } else {
    k1_gemm_loop<false>(lds, lds0, w, rot, src0, src1, step0, xa, wa, acc);
    const bool isq = w < 2;
    const float sc = isq ? QSCALE : 1.f;
#pragma unroll
    for (int a = 0; a < 2; ++a) {
      const int mtile = mt * 2 + a;
      u16* dst = (u16*)(ws + (isq ? WS_Q : WS_K)) + ((((size_t)hb * 64 + mtile) * 2 + hh) * 32 + r) * 8;
#pragma unroll
      for (int s = 0; s < 2; ++s) {
        u32x4 o = {cvtpk(acc[a][8 * s] * sc, acc[a][8 * s + 1] * sc), cvtpk(acc[a][8 * s + 2] * sc, acc[a][8 * s + 3] * sc),
                   cvtpk(acc[a][8 * s + 4] * sc, acc[a][8 * s + 5] * sc), cvtpk(acc[a][8 * s + 6] * sc, acc[a][8 * s + 7] * sc)};
        *(u32x4*)(dst + (size_t)s * 64 * 512) = o;
      }
    }
  }
}

DI void k1_kgen(ldsp lds, const u16* __restrict__ X, const u16* __restrict__ W, const u16* __restrict__ U, const u16* __restrict__ LAR, const u16* __restrict__ AIM, const u16* __restrict__ CR,
                const u16* __restrict__ CI, const u16* __restrict__ LDT, char* __restrict__ ws) {
  const int tid = TIDX, lane = tid & 63, w = rfl(tid >> 6), bid = BIDX;
  const int r = lane & 31;
  const int cp0 = 2 * ((bid & 7) * 32 + (bid >> 3));
  const int ch = w >> 2, pch = cp0 + ch, pi = pch * NST + (w & 3) * 32 + r;
  unsigned plar, paim, pcr, pci, pldt;
  asm volatile("global_load_ushort %0, %1, off" : "=v"(plar) : "v"(LAR + pi) : "memory");
  asm volatile("global_load_ushort %0, %1, off" : "=v"(paim) : "v"(AIM + pi) : "memory");
  asm volatile("global_load_ushort %0, %1, off" : "=v"(pcr) : "v"(CR + pi) : "memory");
  asm volatile("global_load_ushort %0, %1, off" : "=v"(pci) : "v"(CI + pi) : "memory");
  asm volatile("global_load_ushort %0, %1, off" : "=v"(pldt) : "v"(LDT + pch) : "memory");
  const int tl0 = (bid >> 3) * 64, td0 = (bid & 7) * 64 + w * 8;
  u32x4 ua, uc;
  asm volatile("global_load_dwordx4 %0, %1, off" : "=v"(ua) : "v"(U + (size_t)(tl0 + lane) * DM + td0) : "memory");
  asm volatile("global_load_dwordx4 %0, %1, off" : "=v"(uc) : "v"(U + (size_t)(tl0 > 0 ? tl0 - 1 : 0) * DM + td0) : "memory");
  {
    const int xcd = bid & 7, idx = bid >> 3;
    const unsigned dump = (unsigned)(uintptr_t)lds + K1_DUMP;
    if (lane < 48) {
      const int rr = lane / 24, sg = (lane - rr * 24) >> 3, ck = lane & 7;
      glds16((const char*)W + ((size_t)(idx * 16 + w * 2 + rr) * 1536 + sg * 512 + xcd * 64) * 2 + ck * 16, rfl(dump));
    }
  }
  unsigned tix[8];
  const int t8 = tid & 255;
#pragma unroll
  for (int jj = 0; jj < 8; ++jj) {
    const int lag = 2047 - (t8 * 8 + jj);
    const int pos = (int)rbf((float)lag);
    const int a = pos >> 7, e = pos & 7, col = (pos >> 3) & 15;
    const int row = a < 2 ? 8 * a + e : (a < 4 ? 8 + 4 * a + (e >> 1) : (a < 8 ? 16 + 2 * a + (e >> 2) : 24 + a));
    tix[jj] = K1_TAB + ch * 3072 + (row >> 4) * 1024 + (((row >> 2) & 3) * 16 + col) * 16 + (row & 3) * 4;
  }
  asm volatile("s_waitcnt vmcnt(1)" : "+v"(plar), "+v"(paim), "+v"(pcr), "+v"(pci), "+v"(pldt), "+v"(ua), "+v"(uc) :: "memory");
  {
    const float dt = rbf(__builtin_expf(bf2f(pldt)));
    const float ar = rbf(__builtin_expf(bf2f(plar)));
    const float zi = bf2f(paim) * dt, cr = bf2f(pcr) * dt, ci = bf2f(pci) * dt;
    const float HI = 0.15915493667125702f, LO = 6.4206382e-09f;
    const float wh = zi * HI;
    const float wl = __builtin_fmaf(zi, HI, -wh) + zi * LO;
    if (lane < 32) {
      *(LAS f32x4*)(lds + K1_PAR + (w * 32 + lane) * 16) = (f32x4){-(ar * dt) * 1.4426950408889634f, wh, wl, cr};
      *(LAS float*)(lds + K1_PAR2 + (w * 32 + lane) * 4) = ci;
    }
  }
  {
    ldsp tp = lds + K1_TR + w * 1056;
    *(LAS u32x4*)(tp + (lane + 1) * 16) = ua;
    if (lane == 0) {
      if (tl0 == 0) uc = (u32x4){0u, 0u, 0u, 0u};
      *(LAS u32x4*)(tp) = uc;
    }
    const int d = lane >> 3, ls = lane & 7;
    unsigned v[9];
#pragma unroll
    for (int i = 0; i < 9; ++i) v[i] = *(LAS u16*)(tp + (ls * 8 + i) * 16 + d * 2);
    const u32x4 t0 = {v[1] | (v[2] << 16), v[3] | (v[4] << 16), v[5] | (v[6] << 16), v[7] | (v[8] << 16)};
    const u32x4 t1 = {v[0] | (v[1] << 16), v[2] | (v[3] << 16), v[4] | (v[5] << 16), v[6] | (v[7] << 16)};
    const size_t o = (size_t)(td0 + d) * RP + U0 + tl0 + ls * 8;
    *(u32x4*)((u16*)(ws + WS_UT) + o) = t0;
    *(u32x4*)((u16*)(ws + WS_UT1) + o) = t1;
    if (tl0 == SEQ - 64 && ls == 7) *(u32x4*)((u16*)(ws + WS_UT1) + o + 8) = (u32x4){v[8], 0u, 0u, 0u};
  }
  if (tid < 112) {
    const int a = tid / 28, c = tid - a * 28;
    const int c8 = c < 24 ? c : (U0 + SEQ) / 8 + (c - 24);
    u16* dst = (u16*)(ws + ((a & 1) ? WS_UT1 : WS_UT)) + (size_t)(cp0 + (a >> 1)) * RP + c8 * 8;
    if (!((a & 1) && c == 24)) *(u32x4*)dst = (u32x4){0u, 0u, 0u, 0u};
  }
  const int ki = lane & 15, kg = lane >> 4;
  const float ps[4] = {(float)(8 * ki), (float)kbase(0, ki), (float)kbase(1, ki), (float)kbase(2, ki)};
  f32x4 ka[3] = {};
  ldsp pp = lds + K1_PAR + (w * 32 + kg) * 16;
  ldsp pp2 = lds + K1_PAR2 + (w * 32 + kg) * 4;
  kgen_block(pp, pp2, ps, ka);
  kgen_block(pp + 256, pp2 + 64, ps, ka);
#pragma unroll
  for (int T = 0; T < 3; ++T) *(LAS f32x4*)(lds + K1_PART + w * K1_PSZ + (T * 64 + lane) * 16) = ka[T];
  WAITBAR(0);
  if ((w & 3) < 3) {
    const int T = w & 3;
    f32x4 v = *(LAS f32x4*)(lds + K1_PART + (4 * ch) * K1_PSZ + (T * 64 + lane) * 16);
#pragma unroll
    for (int q = 1; q < 4; ++q) v += *(LAS f32x4*)(lds + K1_PART + (4 * ch + q) * K1_PSZ + (T * 64 + lane) * 16);
    *(LAS f32x4*)(lds + K1_TAB + (ch * 3 + T) * 1024 + lane * 16) = v * 2.f;
  }
  LBAR();
  {
    float v[8];
#pragma unroll
    for (int jj = 0; jj < 8; ++jj) v[jj] = *(LAS float*)(lds + tix[jj]);
    u16* kr = (u16*)(ws + WS_KR) + (size_t)pch * RP;
    *(u32x4*)(kr + (4 + t8) * 8) = (u32x4){cvtpk(v[0], v[1]), cvtpk(v[2], v[3]), cvtpk(v[4], v[5]), cvtpk(v[6], v[7])};
    if (t8 < 24) *(u32x4*)(kr + (t8 < 4 ? t8 : 256 + t8) * 8) = (u32x4){0u, 0u, 0u, 0u};
  }
}

__global__ __launch_bounds__(512, 4) void k1_qkv_kgen(const u16* __restrict__ X, const u16* __restrict__ W, const u16* __restrict__ U,
                                                      const u16* __restrict__ LAR, const u16* __restrict__ AIM, const u16* __restrict__ CR,
                                                      const u16* __restrict__ CI, const u16* __restrict__ LDT, char* __restrict__ ws) {
  extern __shared__ __attribute__((aligned(16))) char smem[];
  if (BIDX >= 256) k1_gemm((ldsp)smem, X, W, ws);
  else k1_kgen((ldsp)smem, X, W, U, LAR, AIM, CR, CI, LDT, ws);
}

constexpr int A_ONE = 131072, A_CNT = A_ONE + 256, A_X = A_ONE + 4864, A_LDS = 163840;
constexpr int C_CP = 4480 + 64, C_CH = 4480 + 2 * C_CP, C_UO = 32;

DI void attn_pv(const f32x16& s_in, const bf16x8& v0, const bf16x8& v1, f32x16& o) {
  f32x16 s;
#pragma unroll
  for (int i = 0; i < 8; ++i) s[i] = __builtin_amdgcn_exp2f(s_in[i]);
  const u32x4 p0 = {cvtpk(s[0], s[1]), cvtpk(s[2], s[3]), cvtpk(s[4], s[5]), cvtpk(s[6], s[7])};
  o = MFMA32(v0, __builtin_bit_cast(bf16x8, p0), o);
#pragma unroll
  for (int i = 8; i < 16; ++i) s[i] = __builtin_amdgcn_exp2f(s_in[i]);
  const u32x4 p1 = {cvtpk(s[8], s[9]), cvtpk(s[10], s[11]), cvtpk(s[12], s[13]), cvtpk(s[14], s[15])};
  o = MFMA32(v1, __builtin_bit_cast(bf16x8, p1), o);
}
DI void attn_run(ldsp& kp, ldsp& vp, unsigned vstep, int n, const bf16x8& qf, const f32x16& negm, f32x16& o) {
  f32x16 sa = MFMA32(ldsrd(kp), qf, negm);
#pragma unroll 1
  for (int it = 0; it < n; ++it) {
    const bf16x8 k1 = ldsrd(kp + 4096);
    const bf16x8 va0 = ldsrd(vp), va1 = ldsrd(vp + 512);
    const f32x16 sb = MFMA32(k1, qf, negm);
    const bf16x8 k2 = ldsrd(kp + 8192);
    const bf16x8 vb0 = ldsrd(vp + 4096), vb1 = ldsrd(vp + 4608);
    attn_pv(sa, va0, va1, o);
    sa = MFMA32(k2, qf, negm);
    attn_pv(sb, vb0, vb1, o);
    kp += 8192; vp += vstep;
  }
}
DI bf16x8 rd4(ldsp p) {
  const u32x4 v = {*(LAS unsigned*)p, *(LAS unsigned*)(p + 4), *(LAS unsigned*)(p + 8), *(LAS unsigned*)(p + 12)};
  return __builtin_bit_cast(bf16x8, v);
}
DI void conv_class(ldsp kr, ldsp ut, int rho, f32x4 (&acc)[8]) {
  kr -= 64 * rho; ut -= 64 * rho;
  bf16x8 F[8];
#pragma unroll
  for (int t = 0; t < 8; ++t) F[t] = rd4(ut + 512 * (t + 1));
  if (rho == 0) {
    const bf16x8 fm = rd4(ut);
#pragma unroll
    for (int t = 0; t < 8; ++t) acc[t] = MFMA16(ldsrd(kr - 512 * (t + 1)), fm, acc[t]);
  }
#pragma unroll
  for (int s = 0; s < 8; ++s) {
    const bf16x8 g = ldsrd(kr - 512 * s);
#pragma unroll
    for (int t = s; t < 8; ++t) acc[t] = MFMA16(g, F[t - s], acc[t]);
  }
}
DI void cnt_add(ldsp cnt) {
  asm volatile("" ::: "memory");
  if ((TIDX & 63) == 0) __hip_atomic_fetch_add((LAS unsigned*)cnt, 1u, __ATOMIC_RELAXED, __HIP_MEMORY_SCOPE_WORKGROUP);
}
DI void cnt_wait(ldsp cnt, unsigned target) {
  while (rfl(*(volatile LAS unsigned*)cnt) < target) __builtin_amdgcn_s_sleep(2);
  asm volatile("" ::: "memory");
}
#define WAITL() asm volatile("s_waitcnt lgkmcnt(0)" ::: "memory")
DI float tile_max(const f32x16& s) {
  float m = __builtin_fmaxf(__builtin_fmaxf(s[0], s[1]), __builtin_fmaxf(s[2], s[3]));
#pragma unroll
  for (int i = 4; i < 16; i += 4) m = __builtin_fmaxf(m, __builtin_fmaxf(__builtin_fmaxf(s[i], s[i + 1]), __builtin_fmaxf(s[i + 2], s[i + 3])));
  const auto sw = __builtin_amdgcn_permlane32_swap(F2U(m), F2U(m), false, false);
  return __builtin_fmaxf(U2F(sw[0]), U2F(sw[1]));
}

__global__ __launch_bounds__(1024, 4) void k2_attn(char* __restrict__ ws, const u16* __restrict__ Wo, const u16* __restrict__ DSK,
                                                   u16* __restrict__ out) {
  extern __shared__ __attribute__((aligned(16))) char smem[];
  ldsp lds = (ldsp)smem;
  const unsigned lds0 = (unsigned)(uintptr_t)lds;
  const int tid = TIDX, lane = tid & 63, w = rfl(tid >> 6), bid = BIDX;
  const int h = (bid & 7) * 4 + ((bid >> 3) & 3), qb = bid >> 5;
  const int qt = w & 7, kh = w >> 3, r = lane & 31, hh = lane >> 5;
  bf16x8 qf = *(const bf16x8*)(ws + WS_Q + ((size_t)h * 64 + qb * 8 + qt) * 1024 + lane * 16);
  const bool ldr = w >= 8;
  const char* lsrc = ws + ((w & 2) ? WS_V : WS_K) + (size_t)h * 65536 + (w & 1) * 32768 + ((w >> 2) & 1) * 1024 + lane * 16;
  const unsigned ldst = lds0 + ((w >> 2) & 1) * 4096 + (w & 3) * 1024;
  const int d0 = 2 * ((bid & 7) * 32 + (bid >> 3));
  if (w >= 4) {
    if (w < 8) glds16(lsrc - 1024 + 2048, rfl(ldst - 4096 + 8192));
    else glds16(lsrc, rfl(ldst));
    if (w >= 12) glds16(lsrc + 2048, rfl(ldst + 8192));
  } else if (w == 0) {
    if (lane < 4) *(LAS u32x4*)(lds + A_ONE + (lane & 1) * 512 + (lane >> 1) * 4096) = (u32x4){0x3f803f80u, 0x3f803f80u, 0x3f803f80u, 0x3f803f80u};
    if (lane == 4) *(LAS u32x4*)(lds + A_CNT) = (u32x4){0u, 0u, 0u, 0u};
    if (lane == 5) *(LAS u32x4*)(lds + A_CNT + 16) = (u32x4){0u, 0u, 0u, 0u};
  }
  const int di = r < 4 ? r : (r >= 12 && r < 16 ? r - 8 : (r >= 20 && r < 28 ? r - 12 : -1));
  ldsp const kp0 = lds + kh * 1024 + lane * 16;
  ldsp const vp0 = di >= 0 ? lds + 2048 + kh * 1024 + hh * 256 + di * 16 : lds + A_ONE;
  ldsp kp = kp0, vp = vp0;
  const unsigned vstep = di >= 0 ? 8192u : 0u;
  if (w < 4) {
    const int ch = (w >> 1) & 1, k = w & 1;
    const char* us = ws + (k ? WS_UT1 : WS_UT) + ((size_t)(d0 + ch) * RP + C_UO + lane * 8) * 2;
    const unsigned ud = lds0 + A_X + ch * C_CH + 4480 + k * C_CP;
#pragma unroll
    for (int i = 0; i < 4; ++i) glds16(us + i * 1024, rfl(ud + i * 1024));
    LBAR();
    if (lane < 24) glds16(us + 4096, rfl(ud + 4096));
    const char* ks = ws + WS_KR + ((size_t)(d0 + ch) * RP + k * 1024 + lane * 8) * 2;
    const unsigned kd = lds0 + A_X + ch * C_CH + k * 2048;
    glds16(ks, rfl(kd));
    glds16(ks + 1024, rfl(kd + 1024));
    if (k && lane < 24) glds16(ks + 2048, rfl(kd + 2048));
    WAITV(0);
    cnt_add(lds + A_CNT + 16); cnt_wait(lds + A_CNT + 16, 4);
  } else {
    WAITBAR(0);
  }
  PIN(qf);
  if (ldr) {
#pragma unroll
    for (int i = 2; i < 8; ++i) glds16(lsrc + i * 2048, rfl(ldst + i * 8192));
  }
  if (w < 4) {
    __builtin_amdgcn_s_setprio(3);
    const int cw = w, ch = cw >> 1;
    const int n = lane & 15, g = lane >> 4, c = n & 7;
    ldsp cb = lds + A_X;
    ldsp kr = cb + ch * C_CH + (KX0 + 97 - 8 * n + 8 * g) * 2;
    ldsp ut = cb + ch * C_CH + 4480 + C_CP * (1 - (c & 1)) + (U0 - C_UO + 97 - 256 + 128 * (n >> 3) + c + (1 - (c & 1)) + 8 * g) * 2;
    const unsigned dsk = *(const unsigned*)(DSK + d0);
    const float ds0 = U2F(dsk << 16), ds1 = U2F(dsk & 0xffff0000u);
    f32x4 acc[8] = {};
#pragma unroll 1
    for (int q = 0; q < 4; ++q) conv_class(kr, ut, (cw & 1) + 2 * q, acc);
    const int lo0 = 256 * cw + 128 * (n >> 3) + 32 * g + c;
    float sk0[8], sk1[8];
#pragma unroll
    for (int e = 0; e < 8; ++e) {
      const int l = lo0 + 8 * (e & 3) + 1024 * (e >> 2);
      sk0[e] = rbf(bf2f(*(LAS u16*)(cb + 4480 + (U0 - C_UO + l) * 2)) * ds0);
      sk1[e] = rbf(bf2f(*(LAS u16*)(cb + C_CH + 4480 + (U0 - C_UO + l) * 2)) * ds1);
    }
#pragma unroll
    for (int ph = 0; ph < 2; ++ph) {
      WAITL(); cnt_add(lds + A_CNT + 8); cnt_wait(lds + A_CNT + 8, 8 * ph + 4);
#pragma unroll
      for (int t = 0; t < 4; ++t) *(LAS f32x4*)(cb + ((cw * 4 + t) * 64 + lane) * 16) = acc[4 * ph + t];
      WAITL(); cnt_add(lds + A_CNT + 8); cnt_wait(lds + A_CNT + 8, 8 * ph + 8);
      const f32x4 y0 = *(LAS f32x4*)(cb + (cw * 64 + lane) * 16) + *(LAS f32x4*)(cb + ((4 + cw) * 64 + lane) * 16);
      const f32x4 y1 = *(LAS f32x4*)(cb + ((8 + cw) * 64 + lane) * 16) + *(LAS f32x4*)(cb + ((12 + cw) * 64 + lane) * 16);
#pragma unroll
      for (int e = 0; e < 4; ++e)
        *(unsigned*)(out + (size_t)(lo0 + 8 * e + 1024 * ph) * 1024 + d0) = cvtpk(rbf(y0[e]) + sk0[4 * ph + e], rbf(y1[e]) + sk1[4 * ph + e]);
    }
    WAITL(); cnt_add(lds + A_CNT + 12);
  }
  float m;
  f32x16 negm, o = {};
  {
    const f32x16 z = {};
    m = tile_max(MFMA32(ldsrd(kp), qf, z));
  }
#pragma unroll
  for (int i = 0; i < 16; ++i) negm[i] = -m;
  __builtin_amdgcn_s_setprio(3);
  attn_run(kp, vp, vstep, 1, qf, negm, o);
  if (ldr) {
    WAITV(0);
    cnt_add(lds + A_CNT);
#pragma unroll
    for (int i = 8; i < 16; ++i) glds16(lsrc + i * 2048, rfl(ldst + i * 8192));
  }
  attn_run(kp, vp, vstep, 1, qf, negm, o);
  cnt_wait(lds + A_CNT, 8);
  attn_run(kp, vp, vstep, 2, qf, negm, o);
  if (ldr) {
    WAITV(0);
    cnt_add(lds + A_CNT + 4);
    glds16((const char*)Wo + ((size_t)(bid >> 3) * 16 + (w - 8) * 2) * 1024 + lane * 16, rfl(lds0 + A_ONE + 1024));
    glds16((const char*)Wo + ((size_t)(bid >> 3) * 16 + (w - 8) * 2 + 1) * 1024 + lane * 16, rfl(lds0 + A_ONE + 1024));
  }
  __builtin_amdgcn_s_setprio(2);
  attn_run(kp, vp, vstep, 4, qf, negm, o);
  cnt_wait(lds + A_CNT + 4, 8);
  attn_run(kp, vp, vstep, 2, qf, negm, o);
  __builtin_amdgcn_s_setprio(1);
  attn_run(kp, vp, vstep, 4, qf, negm, o);
  __builtin_amdgcn_s_setprio(0);
  attn_run(kp, vp, vstep, 2, qf, negm, o);
  WAITV(0);
  float l = sel(hh, o[0], o[4]);
  if (__builtin_expect(__builtin_amdgcn_ballot_w64(!(l <= 1.8446744e19f)) != 0, 0)) {
    kp = kp0;
    vp = vp0;
    const unsigned vs1 = vstep >> 1;
    o = (f32x16){};
    const f32x16 z = {};
#pragma unroll 1
    for (int t = 0; t < 32; ++t) {
      f32x16 s = MFMA32(ldsrd(kp), qf, z);
      const bf16x8 v0 = ldsrd(vp), v1 = ldsrd(vp + 512);
      const float mn = __builtin_fmaxf(m, tile_max(s));
      const float al = __builtin_amdgcn_exp2f(m - mn);
      m = mn;
#pragma unroll
      for (int i = 0; i < 16; ++i) { o[i] *= al; s[i] -= mn; }
      attn_pv(s, v0, v1, o);
      kp += 4096; vp += vs1;
    }
    l = sel(hh, o[0], o[4]);
  }
  float od[8];
#pragma unroll
  for (int i = 0; i < 4; ++i) { od[i] = sel(hh, o[4 + i], o[i]); od[4 + i] = sel(hh, o[8 + i], o[12 + i]); }
  ldsp sp = lds + A_X + (qt * 128 + lane) * 16;
  ldsp sq = lds + A_ONE + 2048 + (qt * 32 + r) * 8;
  if (kh == 1) {
    cnt_wait(lds + A_CNT + 12, 4);
    *(LAS f32x4*)(sp) = (f32x4){od[0], od[1], od[2], od[3]};
    *(LAS f32x4*)(sp + 1024) = (f32x4){od[4], od[5], od[6], od[7]};
    if (hh == 0) *(LAS f32x2*)(sq) = (f32x2){m, l};
  }
  LBAR();
  if (kh == 0) {
    const f32x4 a = *(LAS f32x4*)(sp), b = *(LAS f32x4*)(sp + 1024);
    const f32x2 ml = *(LAS f32x2*)(sq);
    const float mm = __builtin_fmaxf(m, ml[0]);
    const float s0 = __builtin_amdgcn_exp2f(m - mm), s1 = __builtin_amdgcn_exp2f(ml[0] - mm);
    const float inv = 1.f / (l * s0 + ml[1] * s1);
    const float f0 = s0 * inv, f1 = s1 * inv;
    float q[8];
#pragma unroll
    for (int i = 0; i < 4; ++i) { q[i] = od[i] * f0 + a[i] * f1; q[4 + i] = od[4 + i] * f0 + b[i] * f1; }
    const u32x4 ov = {cvtpk(q[0], q[1]), cvtpk(q[2], q[3]), cvtpk(q[4], q[5]), cvtpk(q[6], q[7])};
    *(u32x4*)((u16*)(ws + WS_O) + (size_t)(qb * 256 + qt * 32 + r) * DM + h * 16 + hh * 8) = ov;
  }
}

constexpr int K3_LDS = 131072;

__global__ __launch_bounds__(512, 2) void k3_out(const u16* __restrict__ Wo, const char* __restrict__ ws, u16* __restrict__ out) {
  extern __shared__ __attribute__((aligned(16))) char smem[];
  ldsp lds = (ldsp)smem;
  const int tid = TIDX, lane = tid & 63, w = rfl(tid >> 6), bid = BIDX;
  const unsigned lds0 = (unsigned)(uintptr_t)lds;
  const int xcd = bid & 7, idx = bid >> 3;
  const int mt = xcd * 4 + (idx >> 3), nt = idx & 7, m0 = mt * 64, n0 = nt * 64;
  const int r = lane & 31, hh = lane >> 5, wm = w & 1, wn = (w >> 1) & 1, kk = w >> 2;
  const u16* O = (const u16*)(ws + WS_O);
  const int lrow = w * 8 + (lane >> 3), sl = lane & 7;
  const u16* as = O + (size_t)(m0 + lrow) * DM + ((sl ^ ((lrow >> 1) & 7)) << 3);
  const u16* bs = Wo + (size_t)lrow * DM + n0 + ((sl ^ (((lrow >> 1) & 1) << 2)) << 3);
  const int rot = (idx + (idx >> 3)) & 7;
#pragma unroll
  for (int st = 0; st < 8; ++st) {
    const int ks = ((st + rot) & 7) * 64;
    glds16(as + ks, rfl(lds0 + st * 16384 + w * 1024));
    glds16(bs + (size_t)ks * DM, rfl(lds0 + st * 16384 + 8192 + w * 1024));
  }
  unsigned xa[2], wa;
  {
    const int row = 32 * wm + r;
#pragma unroll
    for (int i = 0; i < 2; ++i) xa[i] = row * 128 + (((4 * kk + 2 * i + hh) ^ ((row >> 1) & 7)) << 4);
    const int q = (lane & 15) >> 2, p = lane & 3, blk = (lane >> 4) & 1;
    const unsigned cb = (unsigned)(64 * wn + 32 * blk + 8 * p) ^ (unsigned)(((q >> 1) & 1) << 6);
    wa = 8192 + (32 * kk + 8 * hh + q) * 128 + cb;
  }
  f32x16 acc = {};
#define K3_COMP(st)                                                                              \
  do {                                                                                           \
    ldsp sl_ = lds + (st) * 16384;                                                               \
    bf16x8 xf[2], wf[2];                                                                         \
    _Pragma("unroll") for (int i = 0; i < 2; ++i) {                                              \
      xf[i] = ldsrd(sl_ + xa[i]);                                                                \
      const s16x4 lo = trrd(sl_ + wa + i * 2048);                                                \
      const s16x4 hi = trrd(sl_ + wa + i * 2048 + 512);                                          \
      wf[i] = __builtin_shufflevector(lo, hi, 0, 1, 2, 3, 4, 5, 6, 7);                           \
    }                                                                                            \
    acc = MFMA32(wf[0], xf[0], acc);                                                             \
    acc = MFMA32(wf[1], xf[1], acc);                                                             \
  } while (0)
  WAITBAR(12); K3_COMP(0); K3_COMP(1);
  WAITBAR(8); K3_COMP(2); K3_COMP(3);
  WAITBAR(4); K3_COMP(4); K3_COMP(5);
  WAITBAR(0); K3_COMP(6); K3_COMP(7);
  if (kk == 1) {
#pragma unroll
    for (int q = 0; q < 4; ++q)
      *(LAS f32x4*)(lds + (((w & 3) * 4 + q) * 64 + lane) * 16) = (f32x4){acc[4 * q], acc[4 * q + 1], acc[4 * q + 2], acc[4 * q + 3]};
  }
  LBAR();
  if (kk == 0) {
    ldsp tp = lds + 32768 + (32 * wm + r) * 144 + (32 * wn + 4 * hh) * 2;
#pragma unroll
    for (int q = 0; q < 4; ++q) {
      const f32x4 t = *(LAS f32x4*)(lds + ((w * 4 + q) * 64 + lane) * 16);
      const u32x2 ov = {cvtpk(acc[4 * q] + t[0], acc[4 * q + 1] + t[1]), cvtpk(acc[4 * q + 2] + t[2], acc[4 * q + 3] + t[3])};
      *(LAS u32x2*)(tp + 16 * q) = ov;
    }
  }
  LBAR();
  {
    const int row = tid >> 3, seg = tid & 7;
    const u32x4 v = *(LAS u32x4*)(lds + 32768 + row * 144 + seg * 16);
    *(u32x4*)(out + (size_t)(m0 + row) * 1024 + 512 + n0 + seg * 8) = v;
  }
#undef K3_COMP
}

#if defined(__HIP_DEVICE_COMPILE__)
#pragma clang attribute pop
#endif

extern "C" void kernel_launch(void* const* d_in, const int* in_sizes, int n_in, void* d_out, int out_size, void* d_ws, size_t ws_size,
                              hipStream_t stream) {
  const u16* u = (const u16*)d_in[0];
  const u16* lar = (const u16*)d_in[1];
  const u16* aim = (const u16*)d_in[2];
  const u16* cr = (const u16*)d_in[3];
  const u16* ci = (const u16*)d_in[4];
  const u16* ldt = (const u16*)d_in[5];
  const u16* dsk = (const u16*)d_in[6];
  const u16* x = (const u16*)d_in[7];
  const u16* wqkv = (const u16*)d_in[8];
  const u16* wo = (const u16*)d_in[9];
  (void)hipFuncSetAttribute((const void*)k1_qkv_kgen, hipFuncAttributeMaxDynamicSharedMemorySize, K1_LDS);
  (void)hipFuncSetAttribute((const void*)k2_attn, hipFuncAttributeMaxDynamicSharedMemorySize, A_LDS);
  (void)hipFuncSetAttribute((const void*)k3_out, hipFuncAttributeMaxDynamicSharedMemorySize, K3_LDS);
  k1_qkv_kgen<<<512, 512, K1_LDS, stream>>>(x, wqkv, u, lar, aim, cr, ci, ldt, (char*)d_ws);
  k2_attn<<<256, 1024, A_LDS, stream>>>((char*)d_ws, wo, dsk, (u16*)d_out);
  k3_out<<<256, 512, K3_LDS, stream>>>(wo, (const char*)d_ws, (u16*)d_out);
}
```

```cpp
#include <hip/hip_runtime.h>
#include <stdint.h>

#if defined(__HIP_DEVICE_COMPILE__)
#pragma clang attribute push(__attribute__((target("no-packed-fp32-ops"))), apply_to = function)
#define TIDX ((int)__builtin_amdgcn_workitem_id_x())
#define BIDX ((int)__builtin_amdgcn_workgroup_id_x())
#else
#define TIDX 0
#define BIDX 0
#endif
#define U2F(x) __builtin_bit_cast(float, (unsigned)(x))
#define F2U(x) __builtin_bit_cast(unsigned, (float)(x))

typedef unsigned short u16;
typedef __attribute__((ext_vector_type(8))) short bf16x8;
typedef __attribute__((ext_vector_type(4))) short s16x4;
typedef __attribute__((ext_vector_type(16))) float f32x16;
typedef __attribute__((ext_vector_type(4))) float f32x4;
typedef __attribute__((ext_vector_type(2))) float f32x2;
typedef __attribute__((ext_vector_type(4))) unsigned u32x4;
typedef __attribute__((ext_vector_type(2))) unsigned u32x2;
typedef __attribute__((ext_vector_type(2))) __bf16 bf2_t;
#define DI __device__ __forceinline__
#define LAS __attribute__((address_space(3)))
typedef LAS char* ldsp;

#define MFMA32(a, b, c) __builtin_amdgcn_mfma_f32_32x32x16_bf16((a), (b), (c), 0, 0, 0)
#define MFMA16(a, b, c) __builtin_amdgcn_mfma_f32_16x16x32_bf16((a), (b), (c), 0, 0, 0)
#define MFMAF(a, b, c) __builtin_amdgcn_mfma_f32_16x16x4f32((a), (b), (c), 0, 0, 0)
#define WAITBAR(N) asm volatile("s_waitcnt vmcnt(" #N ") lgkmcnt(0)\n\ts_barrier" ::: "memory")
#define WAITV(N) asm volatile("s_waitcnt vmcnt(" #N ")" ::: "memory")
#define LBAR() asm volatile("s_waitcnt lgkmcnt(0)\n\ts_barrier" ::: "memory")
#define PIN(x) asm volatile("" : "+v"(x))

DI float bf2f(unsigned x) { return U2F(x << 16); }
DI unsigned cvtpk(float a, float b) { f32x2 v = {a, b}; bf2_t r = __builtin_convertvector(v, bf2_t); return __builtin_bit_cast(unsigned, r); }
DI float rbf(float f) { return U2F(cvtpk(f, 0.f) << 16); }
DI float sel(bool c, float a, float b) { PIN(a); PIN(b); return c ? a : b; }
DI void glds16(const void* g, unsigned lds_base) {
  unsigned sv;
  asm volatile("s_mov_b32 %0, m0\n\ts_mov_b32 m0, %2\n\ts_nop 0\n\tglobal_load_lds_dwordx4 %1, off\n\ts_mov_b32 m0, %0"
               : "=&s"(sv) : "v"(g), "s"(lds_base) : "memory");
}
DI s16x4 trrd(ldsp p) { return __builtin_amdgcn_ds_read_tr16_b64_v4i16((LAS s16x4*)p); }
DI bf16x8 ldsrd(ldsp p) { return *(LAS bf16x8*)p; }
DI unsigned rfl(unsigned x) { return __builtin_amdgcn_readfirstlane(x); }

constexpr int SEQ = 2048, DM = 512, NST = 128, NH = 32;
constexpr size_t WS_Q = 0, WS_K = 2u << 20, WS_V = 4u << 20, WS_O = 6u << 20, WS_KR = 8u << 20, WS_UT = 12u << 20, WS_UT1 = 16u << 20;
constexpr int RP = 2560;
constexpr int RU = 2240;
constexpr int RUU = 2272;
constexpr int KX0 = 2079;
constexpr int U0 = 192;
constexpr float QSCALE = 0.25f * 1.4426950408889634f;

constexpr int K1_SLOT = 16384, K1_WOFF = 4096, K1_NS = 5;
constexpr int K1_PSZ = 3 * 256 * 4, K1_PART = 0, K1_PAR = 8 * K1_PSZ, K1_PAR2 = K1_PAR + 4096, K1_TR = K1_PAR2 + 1024, K1_DUMP = K1_TR + 8 * 1056, K1_TAB = K1_DUMP + 1024;
constexpr int K1_LDS = K1_NS * K1_SLOT;
constexpr int CODE_KB = 18;

DI int kbase(int T, int i) {
  if (T == 0) return 128 * (i >> 3) + (i & 7);
  if (T == 1) return i < 8 ? 256 + 128 * (i >> 2) + 2 * (i & 3) : 512 + 128 * ((i - 8) >> 1) + 4 * (i & 1);
  return i < 8 ? 1024 + 128 * i : 2048;
}
DI void kgen_state(ldsp pp, ldsp pp2, const float (&ps)[4], float (&bv)[2], float (&av)[3][2]) {
  const f32x4 pr = *(LAS f32x4*)pp;
  const float cr = pr[3], ci = *(LAS float*)pp2;
  float e[4], f[4], c[4], s[4], k[4];
#pragma unroll
  for (int i = 0; i < 4; ++i) { k[i] = pr[1] * ps[i]; e[i] = pr[0] * ps[i]; }
#pragma unroll
  for (int i = 0; i < 4; ++i) { k[i] = __builtin_rintf(k[i]); e[i] = __builtin_amdgcn_exp2f(e[i]); }
#pragma unroll
  for (int i = 0; i < 4; ++i) f[i] = __builtin_fmaf(pr[1], ps[i], -k[i]);
#pragma unroll
  for (int i = 0; i < 4; ++i) f[i] = __builtin_fmaf(pr[2], ps[i], f[i]);
#pragma unroll
  for (int i = 0; i < 4; ++i) { c[i] = __builtin_amdgcn_cosf(f[i]); s[i] = __builtin_amdgcn_sinf(f[i]); }
  bv[0] = e[0] * c[0]; bv[1] = e[0] * s[0];
#pragma unroll
  for (int T = 0; T < 3; ++T) {
    av[T][0] = __builtin_fmaf(-ci, s[T + 1], cr * c[T + 1]) * e[T + 1];
    av[T][1] = __builtin_fmaf(ci, c[T + 1], cr * s[T + 1]) * -e[T + 1];
  }
}
typedef __attribute__((ext_vector_type(2))) _Float16 h2_t;
typedef __attribute__((ext_vector_type(8))) _Float16 h8_t;
DI unsigned cvth(float a, float b) { f32x2 v = {a, b}; return __builtin_bit_cast(unsigned, __builtin_convertvector(v, h2_t)); }
DI void kgen_block(ldsp pp, ldsp pp2, const float (&ps)[4], f32x4 (&ka)[3]) {
  u32x4 bh, ah[3];
#pragma unroll
  for (int q = 0; q < 4; ++q) {
    float bv[2], av[3][2];
    kgen_state(pp + q * 64, pp2 + q * 16, ps, bv, av);
    bh[q] = cvth(bv[0], bv[1]);
#pragma unroll
    for (int T = 0; T < 3; ++T) ah[T][q] = cvth(av[T][0], av[T][1]);
  }
#pragma unroll
  for (int T = 0; T < 3; ++T)
    ka[T] = __builtin_amdgcn_mfma_f32_16x16x32_f16(__builtin_bit_cast(h8_t, ah[T]), __builtin_bit_cast(h8_t, bh), ka[T], 0, 0, 0);
}

template <bool ISV>
DI void k1_gemm_stage(ldsp sl, const unsigned (&xa)[2][2], unsigned wa, f32x16 (&acc)[2], bool issue, const u16* s0, const u16* s1, unsigned dst) {
  bf16x8 xf[2][2], wf[2];
#pragma unroll
  for (int i = 0; i < 2; ++i) {
    xf[0][i] = ldsrd(sl + xa[0][i]);
    xf[1][i] = ldsrd(sl + xa[1][i]);
    const s16x4 lo = trrd(sl + wa + i * 16 * 384), hi = trrd(sl + wa + i * 16 * 384 + 4 * 384);
    wf[i] = __builtin_shufflevector(lo, hi, 0, 1, 2, 3, 4, 5, 6, 7);
  }
  if (issue) {
    glds16(s0, dst);
    glds16(s1, dst + 8192);
  }
#pragma unroll
  for (int i = 0; i < 2; ++i) {
    if (ISV) { acc[0] = MFMA32(xf[0][i], wf[i], acc[0]); acc[1] = MFMA32(xf[1][i], wf[i], acc[1]); }
    else     { acc[0] = MFMA32(wf[i], xf[0][i], acc[0]); acc[1] = MFMA32(wf[i], xf[1][i], acc[1]); }
  }
}
template <bool ISV>
DI void k1_gemm_loop(ldsp lds, unsigned lds0, int w, int rot, const u16* src0, const u16* src1, size_t step0, const unsigned (&xa)[2][2],
                     unsigned wa, f32x16 (&acc)[2]) {
  WAITBAR(6);
  k1_gemm_stage<ISV>(lds, xa, wa, acc, false, src0, src1, 0);
  unsigned so = K1_SLOT, po = 0;
#pragma unroll 1
  for (int st = 1; st < 12; ++st) {
    WAITBAR(6);
    const int ks = (st + K1_NS - 1 + rot) & 15;
    k1_gemm_stage<ISV>(lds + so, xa, wa, acc, true, src0 + ks * step0, src1 + (size_t)ks * (32 * 1536), rfl(lds0 + po + w * 1024));
    po = so;
    so = so == (K1_NS - 1) * K1_SLOT ? 0 : so + K1_SLOT;
  }
  WAITBAR(6); k1_gemm_stage<ISV>(lds + (12 % K1_NS) * K1_SLOT, xa, wa, acc, false, src0, src1, 0);
  WAITBAR(4); k1_gemm_stage<ISV>(lds + (13 % K1_NS) * K1_SLOT, xa, wa, acc, false, src0, src1, 0);
  WAITBAR(2); k1_gemm_stage<ISV>(lds + (14 % K1_NS) * K1_SLOT, xa, wa, acc, false, src0, src1, 0);
  WAITBAR(0); k1_gemm_stage<ISV>(lds + (15 % K1_NS) * K1_SLOT, xa, wa, acc, false, src0, src1, 0);
}
DI const u16* k1_wsrc(const u16* W, int p, int lane, int xcd) {
  const int off = p * 1024 + lane * 16, row = off / 384, ch = (off - row * 384) >> 4;
  const int cs = ch ^ (((row >> 1) & 1) << 2);
  return W + (size_t)row * 1536 + (cs >> 3) * 512 + xcd * 64 + (cs & 7) * 8;
}

DI void k1_gemm(ldsp lds, const u16* __restrict__ X, const u16* __restrict__ W, char* __restrict__ ws) {
  const unsigned lds0 = (unsigned)(uintptr_t)lds;
  const int tid = TIDX, lane = tid & 63, w = rfl(tid >> 6), bid = BIDX - 256;
  const int xcd = bid & 7, mt = bid >> 3;
  const int m0 = mt * 64;
  const int r = lane & 31, hh = lane >> 5;
  const int rot = 2 * (mt & 7);
  const u16* src0;
  size_t step0;
  if (w < 4) {
    const int row = w * 16 + (lane >> 2);
    src0 = X + (size_t)(m0 + row) * DM + (((lane & 3) ^ ((row >> 2) & 3)) << 3);
    step0 = 32;
  } else {
    src0 = k1_wsrc(W, w - 4, lane, xcd);
    step0 = 32 * 1536;
  }
  const u16* src1 = k1_wsrc(W, w + 4, lane, xcd);
  __builtin_amdgcn_s_sleep(8);
#pragma unroll
  for (int st = 0; st < K1_NS; ++st) {
    const int ks = (st + rot) & 15;
    glds16(src0 + ks * step0, rfl(lds0 + st * K1_SLOT + w * 1024));
    glds16(src1 + (size_t)ks * (32 * 1536), rfl(lds0 + st * K1_SLOT + (8 + w) * 1024));
  }
  __builtin_amdgcn_s_setprio(3);
  if (w >= 6) {
    WAITBAR(6);
    unsigned po = 0;
#pragma unroll 1
    for (int st = 1; st < 12; ++st) {
      WAITBAR(6);
      const int ks = (st + K1_NS - 1 + rot) & 15;
      glds16(src0 + ks * step0, rfl(lds0 + po + w * 1024));
      glds16(src1 + (size_t)ks * (32 * 1536), rfl(lds0 + po + (8 + w) * 1024));
      po = po == (K1_NS - 1) * K1_SLOT ? 0 : po + K1_SLOT;
    }
    WAITBAR(6); WAITBAR(4); WAITBAR(2); WAITBAR(0);
    return;
  }
  unsigned xa[2][2];
#pragma unroll
  for (int a = 0; a < 2; ++a) {
    const int row = 32 * a + r;
#pragma unroll
    for (int i = 0; i < 2; ++i) xa[a][i] = row * 64 + (((2 * i + hh) ^ ((row >> 2) & 3)) << 4);
  }
  const int q = (lane & 15) >> 2;
  const unsigned wa = (unsigned)(K1_WOFF + (8 * hh + q) * 384 + ((64 * w) ^ (((q >> 1) & 1) << 6)) + 32 * ((lane >> 4) & 1) + 8 * (lane & 3));
  f32x16 acc[2] = {};
  const int hb = xcd * 4 + (w & 1) * 2;
  if (w >= 4) {
    k1_gemm_loop<true>(lds, lds0, w, rot, src0, src1, step0, xa, wa, acc);
#pragma unroll
    for (int a = 0; a < 2; ++a) {
      const int mtile = mt * 2 + a;
      const int h = hb + (r >> 4), d = r & 15, si = d < 4 ? d : (d < 8 ? d + 8 : d - 4);
      u16* dst = (u16*)(ws + WS_V) + ((((size_t)h * 128 + 2 * mtile) * 2 + hh) * 16 + si) * 8;
#pragma unroll
      for (int s = 0; s < 2; ++s) {
        u32x4 o = {cvtpk(acc[a][8 * s], acc[a][8 * s + 1]), cvtpk(acc[a][8 * s + 2], acc[a][8 * s + 3]),
                   cvtpk(acc[a][8 * s + 4], acc[a][8 * s + 5]), cvtpk(acc[a][8 * s + 6], acc[a][8 * s + 7])};
        *(u32x4*)(dst + s * 256) = o;
      }
    }
  } else {
    k1_gemm_loop<false>(lds, lds0, w, rot, src0, src1, step0, xa, wa, acc);
    const bool isq = w < 2;
    const float sc = isq ? QSCALE : 1.f;
#pragma unroll
    for (int a = 0; a < 2; ++a) {
      const int mtile = mt * 2 + a;
      u16* dst = (u16*)(ws + (isq ? WS_Q : WS_K)) + ((((size_t)hb * 64 + mtile) * 2 + hh) * 32 + r) * 8;
#pragma unroll
      for (int s = 0; s < 2; ++s) {
        u32x4 o = {cvtpk(acc[a][8 * s] * sc, acc[a][8 * s + 1] * sc), cvtpk(acc[a][8 * s + 2] * sc, acc[a][8 * s + 3] * sc),
                   cvtpk(acc[a][8 * s + 4] * sc, acc[a][8 * s + 5] * sc), cvtpk(acc[a][8 * s + 6] * sc, acc[a][8 * s + 7] * sc)};
        *(u32x4*)(dst + (size_t)s * 64 * 512) = o;
      }
    }
  }
}

DI void k1_kgen(ldsp lds, const u16* __restrict__ X, const u16* __restrict__ W, const u16* __restrict__ U, const u16* __restrict__ LAR, const u16* __restrict__ AIM, const u16* __restrict__ CR,
                const u16* __restrict__ CI, const u16* __restrict__ LDT, char* __restrict__ ws, const u16* __restrict__ Wo,
                const u16* __restrict__ DSK, const u16* __restrict__ out) {
  const int tid = TIDX, lane = tid & 63, w = rfl(tid >> 6), bid = BIDX;
  const int r = lane & 31;
  const int cp0 = 2 * ((bid & 7) * 32 + (bid >> 3));
  const int ch = w >> 2, pch = cp0 + ch, pi = pch * NST + (w & 3) * 32 + r;
  unsigned plar, paim, pcr, pci, pldt;
  asm volatile("global_load_ushort %0, %1, off" : "=v"(plar) : "v"(LAR + pi) : "memory");
  asm volatile("global_load_ushort %0, %1, off" : "=v"(paim) : "v"(AIM + pi) : "memory");
  asm volatile("global_load_ushort %0, %1, off" : "=v"(pcr) : "v"(CR + pi) : "memory");
  asm volatile("global_load_ushort %0, %1, off" : "=v"(pci) : "v"(CI + pi) : "memory");
  asm volatile("global_load_ushort %0, %1, off" : "=v"(pldt) : "v"(LDT + pch) : "memory");
  const int t8 = tid & 255;
  asm volatile("s_waitcnt vmcnt(0)" : "+v"(plar), "+v"(paim), "+v"(pcr), "+v"(pci), "+v"(pldt) :: "memory");
  {
    const int xcd = bid & 7, idx = bid >> 3;
    const unsigned dump = (unsigned)(uintptr_t)lds + K1_DUMP;
    if (lane < 48) {
      const int rr = lane / 24, sg = (lane - rr * 24) >> 3, ck = lane & 7;
      glds16((const char*)W + ((size_t)(idx * 16 + w * 2 + rr) * 1536 + sg * 512 + xcd * 64) * 2 + ck * 16, rfl(dump));
    }
  }
#if defined(__HIP_DEVICE_COMPILE__)
  if (w == 7) {
    const char* c1;
    asm volatile("s_getpc_b64 s[40:41]\n\ts_add_u32 s40, s40, k1_qkv_kgen@rel32@lo+4\n\ts_addc_u32 s41, s41, k1_qkv_kgen@rel32@hi+12\n\ts_mov_b64 %0, s[40:41]"
                 : "=s"(c1) :: "s40", "s41", "scc");
    const int idx = bid >> 3;
    const char* p = c1 + idx * 1024;
    if (idx >= CODE_KB) {
      const int j = idx - CODE_KB;
      p = j < 4 ? (const char*)out + j * (1 << 20) : (j < 6 ? ws + WS_O + (j - 4) * (1 << 20) : (j == 6 ? (const char*)Wo : (const char*)DSK));
    }
    if (idx < CODE_KB + 8) glds16(p + lane * 16, rfl((unsigned)(uintptr_t)lds + K1_DUMP));
  }
#endif
  float cunsc;
  {
    const float dt = rbf(__builtin_expf(bf2f(pldt)));
    const float ar = rbf(__builtin_expf(bf2f(plar)));
    const float zi = bf2f(paim) * dt;
    float cr = bf2f(pcr) * dt, ci = bf2f(pci) * dt;
    unsigned ex = F2U(__builtin_fabsf(cr) + __builtin_fabsf(ci)) >> 23;
#pragma unroll
    for (int d = 1; d < 32; d <<= 1) {
      const unsigned o = (unsigned)__builtin_amdgcn_ds_bpermute((lane ^ d) << 2, (int)ex);
      ex = o > ex ? o : ex;
    }
    ex = ex < 1u ? 1u : (ex > 252u ? 252u : ex);
    const float csc = U2F((253u - ex) << 23);
    cunsc = U2F((ex + 1u) << 23);
    cr *= csc; ci *= csc;
    const float HI = 0.15915493667125702f, LO = 6.4206382e-09f;
    const float wh = zi * HI;
    const float wl = __builtin_fmaf(zi, HI, -wh) + zi * LO;
    if (lane < 32) {
      *(LAS f32x4*)(lds + K1_PAR + (w * 32 + lane) * 16) = (f32x4){-(ar * dt) * 1.4426950408889634f, wh, wl, cr};
      *(LAS float*)(lds + K1_PAR2 + (w * 32 + lane) * 4) = ci;
    }
  }
  const int ki = lane & 15, kg = lane >> 4;
  const float ps[4] = {(float)(8 * ki), (float)kbase(0, ki), (float)kbase(1, ki), (float)kbase(2, ki)};
  f32x4 ka[3] = {};
  ldsp pp = lds + K1_PAR + (w * 32 + kg) * 16;
  ldsp pp2 = lds + K1_PAR2 + (w * 32 + kg) * 4;
#pragma unroll 1
  for (int b = 0; b < 2; ++b) kgen_block(pp + b * 256, pp2 + b * 64, ps, ka);
#pragma unroll
  for (int T = 0; T < 3; ++T) *(LAS f32x4*)(lds + K1_PART + w * K1_PSZ + (T * 64 + lane) * 16) = ka[T] * cunsc;
  WAITBAR(0);
  if ((w & 3) < 3) {
    const int T = w & 3;
    f32x4 v = *(LAS f32x4*)(lds + K1_PART + (4 * ch) * K1_PSZ + (T * 64 + lane) * 16);
#pragma unroll
    for (int q = 1; q < 4; ++q) v += *(LAS f32x4*)(lds + K1_PART + (4 * ch + q) * K1_PSZ + (T * 64 + lane) * 16);
#pragma unroll
    for (int j = 0; j < 4; ++j) {
      const int row = 4 * kg + j;
      const int b0 = 128 * (row >> 3) + (row & 7);
      const int b1 = row < 8 ? 256 + 128 * (row >> 2) + 2 * (row & 3) : 128 * (row >> 1) + 4 * (row & 1);
      const int b2 = 1024 + 128 * row;
      const int pos = (T == 0 ? b0 : (T == 1 ? b1 : b2)) + 8 * ki;
      if (pos <= 2048) *(LAS float*)(lds + K1_TAB + (ch * 2052 + pos) * 4) = 2.f * v[j];
    }
  }
  LBAR();
  {
    float v[8];
#pragma unroll
    for (int jj = 0; jj < 8; ++jj) {
      const int pos = (int)rbf((float)(2047 - (t8 * 8 + jj)));
      v[jj] = *(LAS float*)(lds + K1_TAB + (ch * 2052 + pos) * 4);
    }
    u16* kr = (u16*)(ws + WS_KR) + (size_t)pch * RP;
    *(u32x4*)(kr + (4 + t8) * 8) = (u32x4){cvtpk(v[0], v[1]), cvtpk(v[2], v[3]), cvtpk(v[4], v[5]), cvtpk(v[6], v[7])};
    if (t8 < 24) *(u32x4*)(kr + (t8 < 4 ? t8 : 256 + t8) * 8) = (u32x4){0u, 0u, 0u, 0u};
  }
}

extern "C" __global__ __launch_bounds__(512, 4) void k1_qkv_kgen(const u16* __restrict__ X, const u16* __restrict__ W, const u16* __restrict__ U,
                                                      const u16* __restrict__ LAR, const u16* __restrict__ AIM, const u16* __restrict__ CR,
                                                      const u16* __restrict__ CI, const u16* __restrict__ LDT, char* __restrict__ ws,
                                                      const u16* __restrict__ Wo, const u16* __restrict__ DSK, const u16* __restrict__ out) {
  extern __shared__ __attribute__((aligned(16))) char smem[];
  if (BIDX >= 256) k1_gemm((ldsp)smem, X, W, ws);
  else k1_kgen((ldsp)smem, X, W, U, LAR, AIM, CR, CI, LDT, ws, Wo, DSK, out);
}

constexpr int A_ONE = 131072, A_CNT = A_ONE + 256, A_X = A_ONE + 4864, A_LDS = 163840;
constexpr int C_CP = 4480 + 64, C_CH = 4480 + 2 * C_CP, C_UO = 32;

DI void attn_pv(const f32x16& s_in, const bf16x8& v0, const bf16x8& v1, f32x16& o) {
  f32x16 s;
#pragma unroll
  for (int i = 0; i < 8; ++i) s[i] = __builtin_amdgcn_exp2f(s_in[i]);
  const u32x4 p0 = {cvtpk(s[0], s[1]), cvtpk(s[2], s[3]), cvtpk(s[4], s[5]), cvtpk(s[6], s[7])};
  o = MFMA32(v0, __builtin_bit_cast(bf16x8, p0), o);
#pragma unroll
  for (int i = 8; i < 16; ++i) s[i] = __builtin_amdgcn_exp2f(s_in[i]);
  const u32x4 p1 = {cvtpk(s[8], s[9]), cvtpk(s[10], s[11]), cvtpk(s[12], s[13]), cvtpk(s[14], s[15])};
  o = MFMA32(v1, __builtin_bit_cast(bf16x8, p1), o);
}
DI bf16x8 rd4(ldsp p) {
  const u32x4 v = {*(LAS unsigned*)p, *(LAS unsigned*)(p + 4), *(LAS unsigned*)(p + 8), *(LAS unsigned*)(p + 12)};
  return __builtin_bit_cast(bf16x8, v);
}
DI void conv_class(ldsp kr, ldsp ut, int rho, f32x4 (&acc)[8]) {
  kr -= 64 * rho; ut -= 64 * rho;
  bf16x8 F[8];
#pragma unroll
  for (int t = 0; t < 8; ++t) F[t] = rd4(ut + 512 * (t + 1));
  if (rho == 0) {
    const bf16x8 fm = rd4(ut);
#pragma unroll
    for (int t = 0; t < 8; ++t) acc[t] = MFMA16(ldsrd(kr - 512 * (t + 1)), fm, acc[t]);
  }
#pragma unroll
  for (int s = 0; s < 8; ++s) {
    const bf16x8 g = ldsrd(kr - 512 * s);
#pragma unroll
    for (int t = s; t < 8; ++t) acc[t] = MFMA16(g, F[t - s], acc[t]);
  }
}
DI void cnt_add(ldsp cnt) {
  asm volatile("" ::: "memory");
  if ((TIDX & 63) == 0) __hip_atomic_fetch_add((LAS unsigned*)cnt, 1u, __ATOMIC_RELAXED, __HIP_MEMORY_SCOPE_WORKGROUP);
}
DI void cnt_wait(ldsp cnt, unsigned target) {
  while (rfl(*(volatile LAS unsigned*)cnt) < target) __builtin_amdgcn_s_sleep(2);
  asm volatile("" ::: "memory");
}
#define WAITL() asm volatile("s_waitcnt lgkmcnt(0)" ::: "memory")
DI float tile_max(const f32x16& s) {
  float m = __builtin_fmaxf(__builtin_fmaxf(s[0], s[1]), __builtin_fmaxf(s[2], s[3]));
#pragma unroll
  for (int i = 4; i < 16; i += 4) m = __builtin_fmaxf(m, __builtin_fmaxf(__builtin_fmaxf(s[i], s[i + 1]), __builtin_fmaxf(s[i + 2], s[i + 3])));
  const auto sw = __builtin_amdgcn_permlane32_swap(F2U(m), F2U(m), false, false);
  return __builtin_fmaxf(U2F(sw[0]), U2F(sw[1]));
}

extern "C" __global__ __launch_bounds__(1024, 4) void k2_attn(char* __restrict__ ws, const u16* __restrict__ Wo, const u16* __restrict__ DSK,
                                                   u16* __restrict__ out, const u16* __restrict__ U) {
  extern __shared__ __attribute__((aligned(16))) char smem[];
  ldsp lds = (ldsp)smem;
  const unsigned lds0 = (unsigned)(uintptr_t)lds;
  const int tid = TIDX, lane = tid & 63, w = rfl(tid >> 6), bid = BIDX;
  const int h = (bid & 7) * 4 + ((bid >> 3) & 3), qb = bid >> 5;
  const int qt = w & 7, kh = w >> 3, r = lane & 31, hh = lane >> 5;
  bf16x8 qf = *(const bf16x8*)(ws + WS_Q + ((size_t)h * 64 + qb * 8 + qt) * 1024 + lane * 16);
  const bool ldr = w >= 8;
  const char* lsrc = ws + ((w & 2) ? WS_V : WS_K) + (size_t)h * 65536 + (w & 1) * 32768 + ((w >> 2) & 1) * 1024 + lane * 16;
  const unsigned ldst = lds0 + ((w >> 2) & 1) * 4096 + (w & 3) * 1024;
  const int d0 = 2 * ((bid & 7) * 32 + (bid >> 3));
  if (w >= 4) {
    if (w < 8) glds16(lsrc - 1024 + 2048, rfl(ldst - 4096 + 8192));
    else glds16(lsrc, rfl(ldst));
    if (w >= 12) glds16(lsrc + 2048, rfl(ldst + 8192));
  } else if (w == 0) {
    if (lane < 4) *(LAS u32x4*)(lds + A_ONE + (lane & 1) * 512 + (lane >> 1) * 4096) = (u32x4){0x3f803f80u, 0x3f803f80u, 0x3f803f80u, 0x3f803f80u};
    if (lane == 4) *(LAS u32x4*)(lds + A_CNT) = (u32x4){0u, 0u, 0u, 0u};
    if (lane == 5) *(LAS u32x4*)(lds + A_CNT + 16) = (u32x4){0u, 0u, 0u, 0u};
  }
  const int di = r < 4 ? r : (r >= 12 && r < 16 ? r - 8 : (r >= 20 && r < 28 ? r - 12 : -1));
  ldsp const kp0 = lds + kh * 1024 + lane * 16;
  ldsp const vp0 = di >= 0 ? lds + 2048 + kh * 1024 + hh * 256 + di * 16 : lds + A_ONE;
  ldsp kp = kp0, vp = vp0;
  const unsigned vstep = di >= 0 ? 8192u : 0u;
  if (w < 4) {
#pragma unroll
    for (int i = 0; i < 5; ++i) {
      const int c16 = tid + 256 * i;
      const int ch = c16 >= 568, cc = c16 - 568 * ch;
      if (c16 < 1136) *(LAS u32x4*)(lds + A_X + ch * C_CH + 4480 + cc * 16) = (u32x4){0u, 0u, 0u, 0u};
    }
    LBAR();
    const int ch = w >> 1, k = w & 1;
    const char* ks = ws + WS_KR + ((size_t)(d0 + ch) * RP + k * 1024 + lane * 8) * 2;
    const unsigned kd = lds0 + A_X + ch * C_CH + k * 2048;
    glds16(ks, rfl(kd));
    glds16(ks + 1024, rfl(kd + 1024));
    if (k && lane < 24) glds16(ks + 2048, rfl(kd + 2048));
  } else {
    WAITBAR(0);
  }
  PIN(qf);
  if (ldr) {
#pragma unroll 1
    for (int i = 2; i < 8; ++i) glds16(lsrc + i * 2048, rfl(ldst + i * 8192));
  }
  float m;
  f32x16 negm, o = {};
  {
    const f32x16 z = {};
    m = tile_max(MFMA32(ldsrd(kp), qf, z));
  }
#pragma unroll
  for (int i = 0; i < 16; ++i) negm[i] = -m;
  __builtin_amdgcn_s_setprio(3);
  {
    f32x16 sa = MFMA32(ldsrd(kp), qf, negm);
#pragma unroll 1
    for (int it = 0; it < 16; ++it) {
      const bf16x8 k1 = ldsrd(kp + 4096);
      const bf16x8 va0 = ldsrd(vp), va1 = ldsrd(vp + 512);
      const f32x16 sb = MFMA32(k1, qf, negm);
      const bf16x8 k2 = ldsrd(kp + 8192);
      const bf16x8 vb0 = ldsrd(vp + 4096), vb1 = ldsrd(vp + 4608);
      attn_pv(sa, va0, va1, o);
      sa = MFMA32(k2, qf, negm);
      attn_pv(sb, vb0, vb1, o);
      kp += 8192; vp += vstep;
      if ((0x2249u >> it) & 1u) {
        if (it == 0) {
          if (ldr) {
            WAITV(0);
            cnt_add(lds + A_CNT);
#pragma unroll 1
            for (int i = 8; i < 16; ++i) glds16(lsrc + i * 2048, rfl(ldst + i * 8192));
          }
          cnt_wait(lds + A_CNT, 8);
        } else if (it == 3) {
          if (ldr) {
            WAITV(0);
            cnt_add(lds + A_CNT + 4);
            glds16((const char*)Wo + ((size_t)(bid >> 3) * 16 + (w - 8) * 2) * 1024 + lane * 16, rfl(lds0 + A_ONE + 1024));
            glds16((const char*)Wo + ((size_t)(bid >> 3) * 16 + (w - 8) * 2 + 1) * 1024 + lane * 16, rfl(lds0 + A_ONE + 1024));
          }
          if (w >= 4) __builtin_amdgcn_s_setprio(2);
        } else if (it == 6) {
          cnt_wait(lds + A_CNT + 4, 8);
        } else if (it == 9) {
          if (w >= 4) __builtin_amdgcn_s_setprio(1);
        } else {
          if (w >= 4) __builtin_amdgcn_s_setprio(0);
        }
      }
    }
  }
  WAITV(0);
  float l = sel(hh, o[0], o[4]);
  if (__builtin_expect(__builtin_amdgcn_ballot_w64(!(l <= 1.8446744e19f)) != 0, 0)) {
    kp = kp0;
    vp = vp0;
    const unsigned vs1 = vstep >> 1;
    o = (f32x16){};
    const f32x16 z = {};
#pragma unroll 1
    for (int t = 0; t < 32; ++t) {
      f32x16 s = MFMA32(ldsrd(kp), qf, z);
      const bf16x8 v0 = ldsrd(vp), v1 = ldsrd(vp + 512);
      const float mn = __builtin_fmaxf(m, tile_max(s));
      const float al = __builtin_amdgcn_exp2f(m - mn);
      m = mn;
#pragma unroll
      for (int i = 0; i < 16; ++i) { o[i] *= al; s[i] -= mn; }
      attn_pv(s, v0, v1, o);
      kp += 4096; vp += vs1;
    }
    l = sel(hh, o[0], o[4]);
  }
  float od[8];
#pragma unroll
  for (int i = 0; i < 4; ++i) { od[i] = sel(hh, o[4 + i], o[i]); od[4 + i] = sel(hh, o[8 + i], o[12 + i]); }
  if (w < 4) {
    __builtin_amdgcn_s_setprio(3);
    const int cw = w, ch = cw >> 1;
    const int n = lane & 15, g = lane >> 4, c = n & 7;
    ldsp cb = lds + A_X;
    ldsp kr = cb + ch * C_CH + (KX0 + 97 - 8 * n + 8 * g) * 2;
    ldsp ut = cb + ch * C_CH + 4480 + C_CP * (1 - (c & 1)) + (U0 - C_UO + 97 - 256 + 128 * (n >> 3) + c + (1 - (c & 1)) + 8 * g) * 2;
    const unsigned dsk = *(const unsigned*)(DSK + d0);
    const float ds0 = U2F(dsk << 16), ds1 = U2F(dsk & 0xffff0000u);
    {
      unsigned uv[8];
#pragma unroll
      for (int i = 0; i < 8; ++i) uv[i] = *(const unsigned*)(U + (size_t)(tid + 256 * i) * DM + d0);
#pragma unroll
      for (int i = 0; i < 8; ++i) {
        ldsp p = cb + 4480 + (U0 - C_UO + tid + 256 * i) * 2;
        *(LAS u16*)(p) = (u16)uv[i];
        *(LAS u16*)(p + C_CP + 2) = (u16)uv[i];
        *(LAS u16*)(p + C_CH) = (u16)(uv[i] >> 16);
        *(LAS u16*)(p + C_CH + C_CP + 2) = (u16)(uv[i] >> 16);
      }
    }
    asm volatile("s_waitcnt vmcnt(0) lgkmcnt(0)" ::: "memory");
    cnt_add(lds + A_CNT + 16); cnt_wait(lds + A_CNT + 16, 4);
    f32x4 acc[8] = {};
#pragma unroll 1
    for (int q = 0; q < 4; ++q) conv_class(kr, ut, (cw & 1) + 2 * q, acc);
    const int lo0 = 256 * cw + 128 * (n >> 3) + 32 * g + c;
    float sk0[8], sk1[8];
#pragma unroll
    for (int e = 0; e < 8; ++e) {
      const int l = lo0 + 8 * (e & 3) + 1024 * (e >> 2);
      sk0[e] = rbf(bf2f(*(LAS u16*)(cb + 4480 + (U0 - C_UO + l) * 2)) * ds0);
      sk1[e] = rbf(bf2f(*(LAS u16*)(cb + C_CH + 4480 + (U0 - C_UO + l) * 2)) * ds1);
    }
#pragma unroll
    for (int ph = 0; ph < 2; ++ph) {
      WAITL(); cnt_add(lds + A_CNT + 8); cnt_wait(lds + A_CNT + 8, 8 * ph + 4);
#pragma unroll
      for (int t = 0; t < 4; ++t) *(LAS f32x4*)(cb + ((cw * 4 + t) * 64 + lane) * 16) = acc[4 * ph + t];
      WAITL(); cnt_add(lds + A_CNT + 8); cnt_wait(lds + A_CNT + 8, 8 * ph + 8);
      const f32x4 y0 = *(LAS f32x4*)(cb + (cw * 64 + lane) * 16) + *(LAS f32x4*)(cb + ((4 + cw) * 64 + lane) * 16);
      const f32x4 y1 = *(LAS f32x4*)(cb + ((8 + cw) * 64 + lane) * 16) + *(LAS f32x4*)(cb + ((12 + cw) * 64 + lane) * 16);
#pragma unroll
      for (int e = 0; e < 4; ++e)
        *(unsigned*)(out + (size_t)(lo0 + 8 * e + 1024 * ph) * 1024 + d0) = cvtpk(rbf(y0[e]) + sk0[4 * ph + e], rbf(y1[e]) + sk1[4 * ph + e]);
    }
    WAITL(); cnt_add(lds + A_CNT + 12);
  }
  ldsp sp = lds + A_X + (qt * 128 + lane) * 16;
  ldsp sq = lds + A_ONE + 2048 + (qt * 32 + r) * 8;
  if (kh == 1) {
    cnt_wait(lds + A_CNT + 12, 4);
    *(LAS f32x4*)(sp) = (f32x4){od[0], od[1], od[2], od[3]};
    *(LAS f32x4*)(sp + 1024) = (f32x4){od[4], od[5], od[6], od[7]};
    if (hh == 0) *(LAS f32x2*)(sq) = (f32x2){m, l};
  }
  LBAR();
  if (kh == 0) {
    const f32x4 a = *(LAS f32x4*)(sp), b = *(LAS f32x4*)(sp + 1024);
    const f32x2 ml = *(LAS f32x2*)(sq);
    const float mm = __builtin_fmaxf(m, ml[0]);
    const float s0 = __builtin_amdgcn_exp2f(m - mm), s1 = __builtin_amdgcn_exp2f(ml[0] - mm);
    const float inv = 1.f / (l * s0 + ml[1] * s1);
    const float f0 = s0 * inv, f1 = s1 * inv;
    float q[8];
#pragma unroll
    for (int i = 0; i < 4; ++i) { q[i] = od[i] * f0 + a[i] * f1; q[4 + i] = od[4 + i] * f0 + b[i] * f1; }
    const u32x4 ov = {cvtpk(q[0], q[1]), cvtpk(q[2], q[3]), cvtpk(q[4], q[5]), cvtpk(q[6], q[7])};
    *(u32x4*)((u16*)(ws + WS_O) + (size_t)(qb * 256 + qt * 32 + r) * DM + h * 16 + hh * 8) = ov;
  }
}

constexpr int K3_LDS = 131072;

extern "C" __global__ __launch_bounds__(512, 2) void k3_out(const u16* __restrict__ Wo, const char* __restrict__ ws, u16* __restrict__ out) {
  extern __shared__ __attribute__((aligned(16))) char smem[];
  ldsp lds = (ldsp)smem;
  const int tid = TIDX, lane = tid & 63, w = rfl(tid >> 6), bid = BIDX;
  const unsigned lds0 = (unsigned)(uintptr_t)lds;
  const int xcd = bid & 7, idx = bid >> 3;
  const int mt = xcd * 4 + (idx >> 3), nt = idx & 7, m0 = mt * 64, n0 = nt * 64;
  const int r = lane & 31, hh = lane >> 5, wm = w & 1, wn = (w >> 1) & 1, kk = w >> 2;
  const u16* O = (const u16*)(ws + WS_O);
  const int lrow = w * 8 + (lane >> 3), sl = lane & 7;
  const u16* as = O + (size_t)(m0 + lrow) * DM + ((sl ^ ((lrow >> 1) & 7)) << 3);
  const u16* bs = Wo + (size_t)lrow * DM + n0 + ((sl ^ (((lrow >> 1) & 1) << 2)) << 3);
  const int rot = (idx + (idx >> 3)) & 7;
#pragma unroll
  for (int st = 0; st < 8; ++st) {
    const int ks = ((st + rot) & 7) * 64;
    glds16(as + ks, rfl(lds0 + st * 16384 + w * 1024));
    glds16(bs + (size_t)ks * DM, rfl(lds0 + st * 16384 + 8192 + w * 1024));
  }
  unsigned xa[2], wa;
  {
    const int row = 32 * wm + r;
#pragma unroll
    for (int i = 0; i < 2; ++i) xa[i] = row * 128 + (((4 * kk + 2 * i + hh) ^ ((row >> 1) & 7)) << 4);
    const int q = (lane & 15) >> 2, p = lane & 3, blk = (lane >> 4) & 1;
    const unsigned cb = (unsigned)(64 * wn + 32 * blk + 8 * p) ^ (unsigned)(((q >> 1) & 1) << 6);
    wa = 8192 + (32 * kk + 8 * hh + q) * 128 + cb;
  }
  f32x16 acc = {};
#define K3_COMP(st)                                                                              \
  do {                                                                                           \
    ldsp sl_ = lds + (st) * 16384;                                                               \
    bf16x8 xf[2], wf[2];                                                                         \
    _Pragma("unroll") for (int i = 0; i < 2; ++i) {                                              \
      xf[i] = ldsrd(sl_ + xa[i]);                                                                \
      const s16x4 lo = trrd(sl_ + wa + i * 2048);                                                \
      const s16x4 hi = trrd(sl_ + wa + i * 2048 + 512);                                          \
      wf[i] = __builtin_shufflevector(lo, hi, 0, 1, 2, 3, 4, 5, 6, 7);                           \
    }                                                                                            \
    acc = MFMA32(wf[0], xf[0], acc);                                                             \
    acc = MFMA32(wf[1], xf[1], acc);                                                             \
  } while (0)
  WAITBAR(12); K3_COMP(0); K3_COMP(1);
  WAITBAR(8); K3_COMP(2); K3_COMP(3);
  WAITBAR(4); K3_COMP(4); K3_COMP(5);
  WAITBAR(0); K3_COMP(6); K3_COMP(7);
  if (kk == 1) {
#pragma unroll
    for (int q = 0; q < 4; ++q)
      *(LAS f32x4*)(lds + (((w & 3) * 4 + q) * 64 + lane) * 16) = (f32x4){acc[4 * q], acc[4 * q + 1], acc[4 * q + 2], acc[4 * q + 3]};
  }
  LBAR();
  if (kk == 0) {
    ldsp tp = lds + 32768 + (32 * wm + r) * 144 + (32 * wn + 4 * hh) * 2;
#pragma unroll
    for (int q = 0; q < 4; ++q) {
      const f32x4 t = *(LAS f32x4*)(lds + ((w * 4 + q) * 64 + lane) * 16);
      const u32x2 ov = {cvtpk(acc[4 * q] + t[0], acc[4 * q + 1] + t[1]), cvtpk(acc[4 * q + 2] + t[2], acc[4 * q + 3] + t[3])};
      *(LAS u32x2*)(tp + 16 * q) = ov;
    }
  }
  LBAR();
  {
    const int row = tid >> 3, seg = tid & 7;
    const u32x4 v = *(LAS u32x4*)(lds + 32768 + row * 144 + seg * 16);
    *(u32x4*)(out + (size_t)(m0 + row) * 1024 + 512 + n0 + seg * 8) = v;
  }
#undef K3_COMP
}

#if defined(__HIP_DEVICE_COMPILE__)
#pragma clang attribute pop
#endif

extern "C" void kernel_launch(void* const* d_in, const int* in_sizes, int n_in, void* d_out, int out_size, void* d_ws, size_t ws_size,
                              hipStream_t stream) {
  const u16* u = (const u16*)d_in[0];
  const u16* lar = (const u16*)d_in[1];
  const u16* aim = (const u16*)d_in[2];
  const u16* cr = (const u16*)d_in[3];
  const u16* ci = (const u16*)d_in[4];
  const u16* ldt = (const u16*)d_in[5];
  const u16* dsk = (const u16*)d_in[6];
  const u16* x = (const u16*)d_in[7];
  const u16* wqkv = (const u16*)d_in[8];
  const u16* wo = (const u16*)d_in[9];
  (void)hipFuncSetAttribute((const void*)k1_qkv_kgen, hipFuncAttributeMaxDynamicSharedMemorySize, K1_LDS);
  (void)hipFuncSetAttribute((const void*)k2_attn, hipFuncAttributeMaxDynamicSharedMemorySize, A_LDS);
  (void)hipFuncSetAttribute((const void*)k3_out, hipFuncAttributeMaxDynamicSharedMemorySize, K3_LDS);
  k1_qkv_kgen<<<512, 512, K1_LDS, stream>>>(x, wqkv, u, lar, aim, cr, ci, ldt, (char*)d_ws, wo, dsk, (const u16*)d_out);
  k2_attn<<<256, 1024, A_LDS, stream>>>((char*)d_ws, wo, dsk, (u16*)d_out, u);
  k3_out<<<256, 512, K3_LDS, stream>>>(wo, (const char*)d_ws, (u16*)d_out);
}
```

```cpp
#include <hip/hip_runtime.h>
#include <stdint.h>

#if defined(__HIP_DEVICE_COMPILE__)
#pragma clang attribute push(__attribute__((target("no-packed-fp32-ops"))), apply_to = function)
#define TIDX ((int)__builtin_amdgcn_workitem_id_x())
#define BIDX ((int)__builtin_amdgcn_workgroup_id_x())
#else
#define TIDX 0
#define BIDX 0
#endif
#define U2F(x) __builtin_bit_cast(float, (unsigned)(x))
#define F2U(x) __builtin_bit_cast(unsigned, (float)(x))

typedef unsigned short u16;
typedef __attribute__((ext_vector_type(8))) short bf16x8;
typedef __attribute__((ext_vector_type(4))) short s16x4;
typedef __attribute__((ext_vector_type(16))) float f32x16;
typedef __attribute__((ext_vector_type(4))) float f32x4;
typedef __attribute__((ext_vector_type(2))) float f32x2;
typedef __attribute__((ext_vector_type(4))) unsigned u32x4;
typedef __attribute__((ext_vector_type(2))) unsigned u32x2;
typedef __attribute__((ext_vector_type(2))) __bf16 bf2_t;
#define DI __device__ __forceinline__
#define LAS __attribute__((address_space(3)))
typedef LAS char* ldsp;

#define MFMA32(a, b, c) __builtin_amdgcn_mfma_f32_32x32x16_bf16((a), (b), (c), 0, 0, 0)
#define MFMA16(a, b, c) __builtin_amdgcn_mfma_f32_16x16x32_bf16((a), (b), (c), 0, 0, 0)
#define MFMAF(a, b, c) __builtin_amdgcn_mfma_f32_16x16x4f32((a), (b), (c), 0, 0, 0)
#define WAITBAR(N) asm volatile("s_waitcnt vmcnt(" #N ") lgkmcnt(0)\n\ts_barrier" ::: "memory")
#define WAITV(N) asm volatile("s_waitcnt vmcnt(" #N ")" ::: "memory")
#define LBAR() asm volatile("s_waitcnt lgkmcnt(0)\n\ts_barrier" ::: "memory")
#define PIN(x) asm volatile("" : "+v"(x))

DI float bf2f(unsigned x) { return U2F(x << 16); }
DI unsigned cvtpk(float a, float b) { f32x2 v = {a, b}; bf2_t r = __builtin_convertvector(v, bf2_t); return __builtin_bit_cast(unsigned, r); }
DI float rbf(float f) { return U2F(cvtpk(f, 0.f) << 16); }
DI float sel(bool c, float a, float b) { PIN(a); PIN(b); return c ? a : b; }
DI void glds16(const void* g, unsigned lds_base) {
  unsigned sv;
  asm volatile("s_mov_b32 %0, m0\n\ts_mov_b32 m0, %2\n\ts_nop 0\n\tglobal_load_lds_dwordx4 %1, off\n\ts_mov_b32 m0, %0"
               : "=&s"(sv) : "v"(g), "s"(lds_base) : "memory");
}
DI s16x4 trrd(ldsp p) { return __builtin_amdgcn_ds_read_tr16_b64_v4i16((LAS s16x4*)p); }
DI bf16x8 ldsrd(ldsp p) { return *(LAS bf16x8*)p; }
DI unsigned rfl(unsigned x) { return __builtin_amdgcn_readfirstlane(x); }

constexpr int SEQ = 2048, DM = 512, NST = 128, NH = 32;
constexpr size_t WS_Q = 0, WS_K = 2u << 20, WS_V = 4u << 20, WS_O = 6u << 20, WS_KR = 8u << 20, WS_UT = 12u << 20, WS_UT1 = 16u << 20;
constexpr int RP = 2560;
constexpr int RU = 2240;
constexpr int RUU = 2272;
constexpr int KX0 = 2079;
constexpr int U0 = 192;
constexpr float QSCALE = 0.25f * 1.4426950408889634f;

constexpr int K1_SLOT = 16384, K1_WOFF = 4096, K1_NS = 5;
constexpr int K1_PSZ = 3 * 256 * 4, K1_PART = 0, K1_PAR = 8 * K1_PSZ, K1_PAR2 = K1_PAR + 4096, K1_TR = K1_PAR2 + 1024, K1_DUMP = K1_TR + 8 * 1056, K1_TAB = K1_DUMP + 1024;
constexpr int K1_LDS = K1_NS * K1_SLOT;
constexpr int CODE_KB = 18;

DI int kbase(int T, int i) {
  if (T == 0) return 128 * (i >> 3) + (i & 7);
  if (T == 1) return i < 8 ? 256 + 128 * (i >> 2) + 2 * (i & 3) : 512 + 128 * ((i - 8) >> 1) + 4 * (i & 1);
  return i < 8 ? 1024 + 128 * i : 2048;
}
DI void kgen_state(ldsp pp, ldsp pp2, const float (&ps)[4], float (&bv)[2], float (&av)[3][2]) {
  const f32x4 pr = *(LAS f32x4*)pp;
  const float cr = pr[3], ci = *(LAS float*)pp2;
  float e[4], f[4], c[4], s[4], k[4];
#pragma unroll
  for (int i = 0; i < 4; ++i) { k[i] = pr[1] * ps[i]; e[i] = pr[0] * ps[i]; }
#pragma unroll
  for (int i = 0; i < 4; ++i) { k[i] = __builtin_rintf(k[i]); e[i] = __builtin_amdgcn_exp2f(e[i]); }
#pragma unroll
  for (int i = 0; i < 4; ++i) f[i] = __builtin_fmaf(pr[1], ps[i], -k[i]);
#pragma unroll
  for (int i = 0; i < 4; ++i) f[i] = __builtin_fmaf(pr[2], ps[i], f[i]);
#pragma unroll
  for (int i = 0; i < 4; ++i) { c[i] = __builtin_amdgcn_cosf(f[i]); s[i] = __builtin_amdgcn_sinf(f[i]); }
  bv[0] = e[0] * c[0]; bv[1] = e[0] * s[0];
#pragma unroll
  for (int T = 0; T < 3; ++T) {
    av[T][0] = __builtin_fmaf(-ci, s[T + 1], cr * c[T + 1]) * e[T + 1];
    av[T][1] = __builtin_fmaf(ci, c[T + 1], cr * s[T + 1]) * -e[T + 1];
  }
}
typedef __attribute__((ext_vector_type(2))) _Float16 h2_t;
typedef __attribute__((ext_vector_type(8))) _Float16 h8_t;
DI unsigned cvth(float a, float b) { f32x2 v = {a, b}; return __builtin_bit_cast(unsigned, __builtin_convertvector(v, h2_t)); }
DI void kgen_block(ldsp pp, ldsp pp2, const float (&ps)[4], f32x4 (&ka)[3]) {
  u32x4 bh, ah[3];
#pragma unroll
  for (int q = 0; q < 4; ++q) {
    float bv[2], av[3][2];
    kgen_state(pp + q * 64, pp2 + q * 16, ps, bv, av);
    bh[q] = cvth(bv[0], bv[1]);
#pragma unroll
    for (int T = 0; T < 3; ++T) ah[T][q] = cvth(av[T][0], av[T][1]);
  }
#pragma unroll
  for (int T = 0; T < 3; ++T)
    ka[T] = __builtin_amdgcn_mfma_f32_16x16x32_f16(__builtin_bit_cast(h8_t, ah[T]), __builtin_bit_cast(h8_t, bh), ka[T], 0, 0, 0);
}

template <bool ISV>
DI void k1_gemm_stage(ldsp sl, const unsigned (&xa)[2][2], unsigned wa, f32x16 (&acc)[2], bool issue, const u16* s0, const u16* s1, unsigned dst) {
  bf16x8 xf[2][2], wf[2];
#pragma unroll
  for (int i = 0; i < 2; ++i) {
    xf[0][i] = ldsrd(sl + xa[0][i]);
    xf[1][i] = ldsrd(sl + xa[1][i]);
    const s16x4 lo = trrd(sl + wa + i * 16 * 384), hi = trrd(sl + wa + i * 16 * 384 + 4 * 384);
    wf[i] = __builtin_shufflevector(lo, hi, 0, 1, 2, 3, 4, 5, 6, 7);
  }
  if (issue) {
    glds16(s0, dst);
    glds16(s1, dst + 8192);
  }
#pragma unroll
  for (int i = 0; i < 2; ++i) {
    if (ISV) { acc[0] = MFMA32(xf[0][i], wf[i], acc[0]); acc[1] = MFMA32(xf[1][i], wf[i], acc[1]); }
    else     { acc[0] = MFMA32(wf[i], xf[0][i], acc[0]); acc[1] = MFMA32(wf[i], xf[1][i], acc[1]); }
  }
}
template <bool ISV>
DI void k1_gemm_loop(ldsp lds, unsigned lds0, int w, int rot, const u16* src0, const u16* src1, size_t step0, const unsigned (&xa)[2][2],
                     unsigned wa, f32x16 (&acc)[2]) {
  WAITBAR(6);
  k1_gemm_stage<ISV>(lds, xa, wa, acc, false, src0, src1, 0);
  unsigned so = K1_SLOT, po = 0;
#pragma unroll 1
  for (int st = 1; st < 12; ++st) {
    WAITBAR(6);
    const int ks = (st + K1_NS - 1 + rot) & 15;
    k1_gemm_stage<ISV>(lds + so, xa, wa, acc, true, src0 + ks * step0, src1 + (size_t)ks * (32 * 1536), rfl(lds0 + po + w * 1024));
    po = so;
    so = so == (K1_NS - 1) * K1_SLOT ? 0 : so + K1_SLOT;
  }
  WAITBAR(6); k1_gemm_stage<ISV>(lds + (12 % K1_NS) * K1_SLOT, xa, wa, acc, false, src0, src1, 0);
  WAITBAR(4); k1_gemm_stage<ISV>(lds + (13 % K1_NS) * K1_SLOT, xa, wa, acc, false, src0, src1, 0);
  WAITBAR(2); k1_gemm_stage<ISV>(lds + (14 % K1_NS) * K1_SLOT, xa, wa, acc, false, src0, src1, 0);
  WAITBAR(0); k1_gemm_stage<ISV>(lds + (15 % K1_NS) * K1_SLOT, xa, wa, acc, false, src0, src1, 0);
}
DI const u16* k1_wsrc(const u16* W, int p, int lane, int xcd) {
  const int off = p * 1024 + lane * 16, row = off / 384, ch = (off - row * 384) >> 4;
  const int cs = ch ^ (((row >> 1) & 1) << 2);
  return W + (size_t)row * 1536 + (cs >> 3) * 512 + xcd * 64 + (cs & 7) * 8;
}

DI void k1_gemm(ldsp lds, const u16* __restrict__ X, const u16* __restrict__ W, char* __restrict__ ws) {
  const unsigned lds0 = (unsigned)(uintptr_t)lds;
  const int tid = TIDX, lane = tid & 63, w = rfl(tid >> 6), bid = BIDX - 256;
  const int xcd = bid & 7, mt = bid >> 3;
  const int m0 = mt * 64;
  const int r = lane & 31, hh = lane >> 5;
  const int rot = 2 * (mt & 7);
  const u16* src0;
  size_t step0;
  if (w < 4) {
    const int row = w * 16 + (lane >> 2);
    src0 = X + (size_t)(m0 + row) * DM + (((lane & 3) ^ ((row >> 2) & 3)) << 3);
    step0 = 32;
  } else {
    src0 = k1_wsrc(W, w - 4, lane, xcd);
    step0 = 32 * 1536;
  }
  const u16* src1 = k1_wsrc(W, w + 4, lane, xcd);
  __builtin_amdgcn_s_sleep(8);
#pragma unroll
  for (int st = 0; st < K1_NS; ++st) {
    const int ks = (st + rot) & 15;
    glds16(src0 + ks * step0, rfl(lds0 + st * K1_SLOT + w * 1024));
    glds16(src1 + (size_t)ks * (32 * 1536), rfl(lds0 + st * K1_SLOT + (8 + w) * 1024));
  }
  __builtin_amdgcn_s_setprio(3);
  if (w >= 6) {
    WAITBAR(6);
    unsigned po = 0;
#pragma unroll 1
    for (int st = 1; st < 12; ++st) {
      WAITBAR(6);
      const int ks = (st + K1_NS - 1 + rot) & 15;
      glds16(src0 + ks * step0, rfl(lds0 + po + w * 1024));
      glds16(src1 + (size_t)ks * (32 * 1536), rfl(lds0 + po + (8 + w) * 1024));
      po = po == (K1_NS - 1) * K1_SLOT ? 0 : po + K1_SLOT;
    }
    WAITBAR(6); WAITBAR(4); WAITBAR(2); WAITBAR(0);
    return;
  }
  unsigned xa[2][2];
#pragma unroll
  for (int a = 0; a < 2; ++a) {
    const int row = 32 * a + r;
#pragma unroll
    for (int i = 0; i < 2; ++i) xa[a][i] = row * 64 + (((2 * i + hh) ^ ((row >> 2) & 3)) << 4);
  }
  const int q = (lane & 15) >> 2;
  const unsigned wa = (unsigned)(K1_WOFF + (8 * hh + q) * 384 + ((64 * w) ^ (((q >> 1) & 1) << 6)) + 32 * ((lane >> 4) & 1) + 8 * (lane & 3));
  f32x16 acc[2] = {};
  const int hb = xcd * 4 + (w & 1) * 2;
  if (w >= 4) {
    k1_gemm_loop<true>(lds, lds0, w, rot, src0, src1, step0, xa, wa, acc);
#pragma unroll
    for (int a = 0; a < 2; ++a) {
      const int mtile = mt * 2 + a;
      const int h = hb + (r >> 4), d = r & 15, si = d < 4 ? d : (d < 8 ? d + 8 : d - 4);
      u16* dst = (u16*)(ws + WS_V) + ((((size_t)h * 128 + 2 * mtile) * 2 + hh) * 16 + si) * 8;
#pragma unroll
      for (int s = 0; s < 2; ++s) {
        u32x4 o = {cvtpk(acc[a][8 * s], acc[a][8 * s + 1]), cvtpk(acc[a][8 * s + 2], acc[a][8 * s + 3]),
                   cvtpk(acc[a][8 * s + 4], acc[a][8 * s + 5]), cvtpk(acc[a][8 * s + 6], acc[a][8 * s + 7])};
        *(u32x4*)(dst + s * 256) = o;
      }
    }
  } else {
    k1_gemm_loop<false>(lds, lds0, w, rot, src0, src1, step0, xa, wa, acc);
    const bool isq = w < 2;
    const float sc = isq ? QSCALE : 1.f;
#pragma unroll
    for (int a = 0; a < 2; ++a) {
      const int mtile = mt * 2 + a;
      u16* dst = (u16*)(ws + (isq ? WS_Q : WS_K)) + ((((size_t)hb * 64 + mtile) * 2 + hh) * 32 + r) * 8;
#pragma unroll
      for (int s = 0; s < 2; ++s) {
        u32x4 o = {cvtpk(acc[a][8 * s] * sc, acc[a][8 * s + 1] * sc), cvtpk(acc[a][8 * s + 2] * sc, acc[a][8 * s + 3] * sc),
                   cvtpk(acc[a][8 * s + 4] * sc, acc[a][8 * s + 5] * sc), cvtpk(acc[a][8 * s + 6] * sc, acc[a][8 * s + 7] * sc)};
        *(u32x4*)(dst + (size_t)s * 64 * 512) = o;
      }
    }
  }
}

DI void k1_kgen(ldsp lds, const u16* __restrict__ X, const u16* __restrict__ W, const u16* __restrict__ U, const u16* __restrict__ LAR, const u16* __restrict__ AIM, const u16* __restrict__ CR,
                const u16* __restrict__ CI, const u16* __restrict__ LDT, char* __restrict__ ws, const u16* __restrict__ Wo,
                const u16* __restrict__ DSK, const u16* __restrict__ out) {
  const int tid = TIDX, lane = tid & 63, w = rfl(tid >> 6), bid = BIDX;
  const int r = lane & 31;
  const int cp0 = 2 * ((bid & 7) * 32 + (bid >> 3));
  const int ch = w >> 2, pch = cp0 + ch, pi = pch * NST + (w & 3) * 32 + r;
  unsigned plar, paim, pcr, pci, pldt;
  asm volatile("global_load_ushort %0, %1, off" : "=v"(plar) : "v"(LAR + pi) : "memory");
  asm volatile("global_load_ushort %0, %1, off" : "=v"(paim) : "v"(AIM + pi) : "memory");
  asm volatile("global_load_ushort %0, %1, off" : "=v"(pcr) : "v"(CR + pi) : "memory");
  asm volatile("global_load_ushort %0, %1, off" : "=v"(pci) : "v"(CI + pi) : "memory");
  asm volatile("global_load_ushort %0, %1, off" : "=v"(pldt) : "v"(LDT + pch) : "memory");
  const int t8 = tid & 255;
  asm volatile("s_waitcnt vmcnt(0)" : "+v"(plar), "+v"(paim), "+v"(pcr), "+v"(pci), "+v"(pldt) :: "memory");
  {
    const int xcd = bid & 7, idx = bid >> 3;
    const unsigned dump = (unsigned)(uintptr_t)lds + K1_DUMP;
    if (lane < 48) {
      const int rr = lane / 24, sg = (lane - rr * 24) >> 3, ck = lane & 7;
      glds16((const char*)W + ((size_t)(idx * 16 + w * 2 + rr) * 1536 + sg * 512 + xcd * 64) * 2 + ck * 16, rfl(dump));
    }
  }
#if defined(__HIP_DEVICE_COMPILE__)
  if (w == 7) {
    const char* c1;
    asm volatile("s_getpc_b64 s[40:41]\n\ts_add_u32 s40, s40, k1_qkv_kgen@rel32@lo+4\n\ts_addc_u32 s41, s41, k1_qkv_kgen@rel32@hi+12\n\ts_mov_b64 %0, s[40:41]"
                 : "=s"(c1) :: "s40", "s41", "scc");
    const int idx = bid >> 3;
    const char* p = c1 + idx * 1024;
    if (idx >= CODE_KB) {
      const int j = idx - CODE_KB;
      p = j < 4 ? (const char*)out + j * (1 << 20) : (j < 6 ? ws + WS_O + (j - 4) * (1 << 20) : (j == 6 ? (const char*)Wo : (const char*)DSK));
    }
    if (idx < CODE_KB + 8) glds16(p + lane * 16, rfl((unsigned)(uintptr_t)lds + K1_DUMP));
  }
#endif
  float cunsc;
  {
    const float dt = rbf(__builtin_expf(bf2f(pldt)));
    const float ar = rbf(__builtin_expf(bf2f(plar)));
    const float zi = bf2f(paim) * dt;
    float cr = bf2f(pcr) * dt, ci = bf2f(pci) * dt;
    unsigned ex = F2U(__builtin_fabsf(cr) + __builtin_fabsf(ci)) >> 23;
#pragma unroll
    for (int d = 1; d < 32; d <<= 1) {
      const unsigned o = (unsigned)__builtin_amdgcn_ds_bpermute((lane ^ d) << 2, (int)ex);
      ex = o > ex ? o : ex;
    }
    ex = ex < 1u ? 1u : (ex > 252u ? 252u : ex);
    const float csc = U2F((253u - ex) << 23);
    cunsc = U2F((ex + 1u) << 23);
    cr *= csc; ci *= csc;
    const float HI = 0.15915493667125702f, LO = 6.4206382e-09f;
    const float wh = zi * HI;
    const float wl = __builtin_fmaf(zi, HI, -wh) + zi * LO;
    if (lane < 32) {
      *(LAS f32x4*)(lds + K1_PAR + (w * 32 + lane) * 16) = (f32x4){-(ar * dt) * 1.4426950408889634f, wh, wl, cr};
      *(LAS float*)(lds + K1_PAR2 + (w * 32 + lane) * 4) = ci;
    }
  }
  const int ki = lane & 15, kg = lane >> 4;
  const float ps[4] = {(float)(8 * ki), (float)kbase(0, ki), (float)kbase(1, ki), (float)kbase(2, ki)};
  f32x4 ka[3] = {};
  ldsp pp = lds + K1_PAR + (w * 32 + kg) * 16;
  ldsp pp2 = lds + K1_PAR2 + (w * 32 + kg) * 4;
#pragma unroll 1
  for (int b = 0; b < 2; ++b) kgen_block(pp + b * 256, pp2 + b * 64, ps, ka);
#pragma unroll
  for (int T = 0; T < 3; ++T) *(LAS f32x4*)(lds + K1_PART + w * K1_PSZ + (T * 64 + lane) * 16) = ka[T] * cunsc;
  WAITBAR(0);
  if ((w & 3) < 3) {
    const int T = w & 3;
    f32x4 v = *(LAS f32x4*)(lds + K1_PART + (4 * ch) * K1_PSZ + (T * 64 + lane) * 16);
#pragma unroll
    for (int q = 1; q < 4; ++q) v += *(LAS f32x4*)(lds + K1_PART + (4 * ch + q) * K1_PSZ + (T * 64 + lane) * 16);
#pragma unroll
    for (int j = 0; j < 4; ++j) {
      const int row = 4 * kg + j;
      const int b0 = 128 * (row >> 3) + (row & 7);
      const int b1 = row < 8 ? 256 + 128 * (row >> 2) + 2 * (row & 3) : 128 * (row >> 1) + 4 * (row & 1);
      const int b2 = 1024 + 128 * row;
      const int pos = (T == 0 ? b0 : (T == 1 ? b1 : b2)) + 8 * ki;
      if (pos <= 2048) *(LAS float*)(lds + K1_TAB + (ch * 2052 + pos) * 4) = 2.f * v[j];
    }
  }
  LBAR();
  {
    float v[8];
#pragma unroll
    for (int jj = 0; jj < 8; ++jj) {
      const int pos = (int)rbf((float)(2047 - (t8 * 8 + jj)));
      v[jj] = *(LAS float*)(lds + K1_TAB + (ch * 2052 + pos) * 4);
    }
    u16* kr = (u16*)(ws + WS_KR) + (size_t)pch * RP;
    *(u32x4*)(kr + (4 + t8) * 8) = (u32x4){cvtpk(v[0], v[1]), cvtpk(v[2], v[3]), cvtpk(v[4], v[5]), cvtpk(v[6], v[7])};
    if (t8 < 24) *(u32x4*)(kr + (t8 < 4 ? t8 : 256 + t8) * 8) = (u32x4){0u, 0u, 0u, 0u};
  }
}

extern "C" __global__ __launch_bounds__(512, 4) void k1_qkv_kgen(const u16* __restrict__ X, const u16* __restrict__ W, const u16* __restrict__ U,
                                                      const u16* __restrict__ LAR, const u16* __restrict__ AIM, const u16* __restrict__ CR,
                                                      const u16* __restrict__ CI, const u16* __restrict__ LDT, char* __restrict__ ws,
                                                      const u16* __restrict__ Wo, const u16* __restrict__ DSK, const u16* __restrict__ out) {
  extern __shared__ __attribute__((aligned(16))) char smem[];
  if (BIDX >= 256) k1_gemm((ldsp)smem, X, W, ws);
  else k1_kgen((ldsp)smem, X, W, U, LAR, AIM, CR, CI, LDT, ws, Wo, DSK, out);
}

constexpr int A_ONE = 131072, A_CNT = A_ONE + 256, A_X = A_ONE + 4864, A_LDS = 163840;
constexpr int C_CP = 4480 + 64, C_CH = 4480 + 2 * C_CP, C_UO = 32;

DI void attn_pv(const f32x16& s_in, const bf16x8& v0, const bf16x8& v1, f32x16& o) {
  f32x16 s;
#pragma unroll
  for (int i = 0; i < 8; ++i) s[i] = __builtin_amdgcn_exp2f(s_in[i]);
  const u32x4 p0 = {cvtpk(s[0], s[1]), cvtpk(s[2], s[3]), cvtpk(s[4], s[5]), cvtpk(s[6], s[7])};
  o = MFMA32(v0, __builtin_bit_cast(bf16x8, p0), o);
#pragma unroll
  for (int i = 8; i < 16; ++i) s[i] = __builtin_amdgcn_exp2f(s_in[i]);
  const u32x4 p1 = {cvtpk(s[8], s[9]), cvtpk(s[10], s[11]), cvtpk(s[12], s[13]), cvtpk(s[14], s[15])};
  o = MFMA32(v1, __builtin_bit_cast(bf16x8, p1), o);
}
DI bf16x8 rd4(ldsp p) {
  const u32x4 v = {*(LAS unsigned*)p, *(LAS unsigned*)(p + 4), *(LAS unsigned*)(p + 8), *(LAS unsigned*)(p + 12)};
  return __builtin_bit_cast(bf16x8, v);
}
DI void conv_class(ldsp kr, ldsp ut, int rho, f32x4 (&acc)[8]) {
  kr -= 64 * rho; ut -= 64 * rho;
  bf16x8 F[8];
#pragma unroll
  for (int t = 0; t < 8; ++t) F[t] = rd4(ut + 512 * (t + 1));
  if (rho == 0) {
    const bf16x8 fm = rd4(ut);
#pragma unroll
    for (int t = 0; t < 8; ++t) acc[t] = MFMA16(ldsrd(kr - 512 * (t + 1)), fm, acc[t]);
  }
#pragma unroll
  for (int s = 0; s < 8; ++s) {
    const bf16x8 g = ldsrd(kr - 512 * s);
#pragma unroll
    for (int t = s; t < 8; ++t) acc[t] = MFMA16(g, F[t - s], acc[t]);
  }
}
DI void cnt_add(ldsp cnt) {
  asm volatile("" ::: "memory");
  if ((TIDX & 63) == 0) __hip_atomic_fetch_add((LAS unsigned*)cnt, 1u, __ATOMIC_RELAXED, __HIP_MEMORY_SCOPE_WORKGROUP);
}
DI void cnt_wait(ldsp cnt, unsigned target) {
  while (rfl(*(volatile LAS unsigned*)cnt) < target) __builtin_amdgcn_s_sleep(2);
  asm volatile("" ::: "memory");
}
#define WAITL() asm volatile("s_waitcnt lgkmcnt(0)" ::: "memory")
DI float tile_max(const f32x16& s) {
  float m = __builtin_fmaxf(__builtin_fmaxf(s[0], s[1]), __builtin_fmaxf(s[2], s[3]));
#pragma unroll
  for (int i = 4; i < 16; i += 4) m = __builtin_fmaxf(m, __builtin_fmaxf(__builtin_fmaxf(s[i], s[i + 1]), __builtin_fmaxf(s[i + 2], s[i + 3])));
  const auto sw = __builtin_amdgcn_permlane32_swap(F2U(m), F2U(m), false, false);
  return __builtin_fmaxf(U2F(sw[0]), U2F(sw[1]));
}

extern "C" __global__ __launch_bounds__(1024, 4) void k2_attn(char* __restrict__ ws, const u16* __restrict__ Wo, const u16* __restrict__ DSK,
                                                   u16* __restrict__ out, const u16* __restrict__ U) {
  extern __shared__ __attribute__((aligned(16))) char smem[];
  ldsp lds = (ldsp)smem;
  const unsigned lds0 = (unsigned)(uintptr_t)lds;
  const int tid = TIDX, lane = tid & 63, w = rfl(tid >> 6), bid = BIDX;
  const int h = (bid & 7) * 4 + ((bid >> 3) & 3), qb = bid >> 5;
  const int qt = w & 7, kh = w >> 3, r = lane & 31, hh = lane >> 5;
  bf16x8 qf = *(const bf16x8*)(ws + WS_Q + ((size_t)h * 64 + qb * 8 + qt) * 1024 + lane * 16);
  const bool ldr = w >= 8;
  const char* lsrc = ws + ((w & 2) ? WS_V : WS_K) + (size_t)h * 65536 + (w & 1) * 32768 + ((w >> 2) & 1) * 1024 + lane * 16;
  const unsigned ldst = lds0 + ((w >> 2) & 1) * 4096 + (w & 3) * 1024;
  const int d0 = 2 * ((bid & 7) * 32 + (bid >> 3));
  if (w >= 4) {
    if (w < 8) glds16(lsrc - 1024 + 2048, rfl(ldst - 4096 + 8192));
    else glds16(lsrc, rfl(ldst));
    if (w >= 12) glds16(lsrc + 2048, rfl(ldst + 8192));
  } else if (w == 0) {
    if (lane < 4) *(LAS u32x4*)(lds + A_ONE + (lane & 1) * 512 + (lane >> 1) * 4096) = (u32x4){0x3f803f80u, 0x3f803f80u, 0x3f803f80u, 0x3f803f80u};
    if (lane == 4) *(LAS u32x4*)(lds + A_CNT) = (u32x4){0u, 0u, 0u, 0u};
    if (lane == 5) *(LAS u32x4*)(lds + A_CNT + 16) = (u32x4){0u, 0u, 0u, 0u};
  }
  const int di = r < 4 ? r : (r >= 12 && r < 16 ? r - 8 : (r >= 20 && r < 28 ? r - 12 : -1));
  ldsp const kp0 = lds + kh * 1024 + lane * 16;
  ldsp const vp0 = di >= 0 ? lds + 2048 + kh * 1024 + hh * 256 + di * 16 : lds + A_ONE;
  ldsp kp = kp0, vp = vp0;
  const unsigned vstep = di >= 0 ? 8192u : 0u;
  if (w < 4) {
#pragma unroll
    for (int i = 0; i < 5; ++i) {
      const int c16 = tid + 256 * i;
      const int ch = c16 >= 568, cc = c16 - 568 * ch;
      if (c16 < 1136) *(LAS u32x4*)(lds + A_X + ch * C_CH + 4480 + cc * 16) = (u32x4){0u, 0u, 0u, 0u};
    }
    LBAR();
    const int ch = w >> 1, k = w & 1;
    const char* ks = ws + WS_KR + ((size_t)(d0 + ch) * RP + k * 1024 + lane * 8) * 2;
    const unsigned kd = lds0 + A_X + ch * C_CH + k * 2048;
    glds16(ks, rfl(kd));
    glds16(ks + 1024, rfl(kd + 1024));
    if (k && lane < 24) glds16(ks + 2048, rfl(kd + 2048));
  } else {
    WAITBAR(0);
  }
  PIN(qf);
  if (ldr) {
#pragma unroll 1
    for (int i = 2; i < 8; ++i) glds16(lsrc + i * 2048, rfl(ldst + i * 8192));
  }
  float m;
  f32x16 negm, o = {};
  {
    const f32x16 z = {};
    m = tile_max(MFMA32(ldsrd(kp), qf, z));
  }
#pragma unroll
  for (int i = 0; i < 16; ++i) negm[i] = -m;
  if (w >= 4 && w < 8) __builtin_amdgcn_s_setprio(2); else __builtin_amdgcn_s_setprio(3);
  {
    f32x16 sa = MFMA32(ldsrd(kp), qf, negm);
#pragma unroll 1
    for (int it = 0; it < 16; ++it) {
      const bf16x8 k1 = ldsrd(kp + 4096);
      const bf16x8 va0 = ldsrd(vp), va1 = ldsrd(vp + 512);
      const f32x16 sb = MFMA32(k1, qf, negm);
      const bf16x8 k2 = ldsrd(kp + 8192);
      const bf16x8 vb0 = ldsrd(vp + 4096), vb1 = ldsrd(vp + 4608);
      attn_pv(sa, va0, va1, o);
      sa = MFMA32(k2, qf, negm);
      attn_pv(sb, vb0, vb1, o);
      kp += 8192; vp += vstep;
      if ((0x224Bu >> it) & 1u) {
        if (it == 0) {
          if (ldr) {
            WAITV(0);
            cnt_add(lds + A_CNT);
#pragma unroll 1
            for (int i = 8; i < 16; ++i) glds16(lsrc + i * 2048, rfl(ldst + i * 8192));
          }
          cnt_wait(lds + A_CNT, 8);
        } else if (it == 1) {
          if (ldr) {
            WAITV(0);
            cnt_add(lds + A_CNT + 4);
            glds16((const char*)U + ((size_t)(bid >> 3) * 64 + (w - 8) * 8 + (lane >> 3)) * 1024 + (bid & 7) * 128 + (lane & 7) * 16,
                   rfl(lds0 + A_ONE + 1024));
            glds16((const char*)Wo + ((size_t)(bid >> 3) * 16 + (w - 8) * 2) * 1024 + lane * 16, rfl(lds0 + A_ONE + 1024));
            glds16((const char*)Wo + ((size_t)(bid >> 3) * 16 + (w - 8) * 2 + 1) * 1024 + lane * 16, rfl(lds0 + A_ONE + 1024));
          }
        } else if (it == 3) {
          if (w >= 4) __builtin_amdgcn_s_setprio(2);
        } else if (it == 6) {
          cnt_wait(lds + A_CNT + 4, 8);
        } else if (it == 9) {
          if (w >= 4) __builtin_amdgcn_s_setprio(1);
        } else {
          if (w >= 4) __builtin_amdgcn_s_setprio(0);
        }
      }
    }
  }
  WAITV(0);
  float l = sel(hh, o[0], o[4]);
  if (__builtin_expect(__builtin_amdgcn_ballot_w64(!(l <= 1.8446744e19f)) != 0, 0)) {
    kp = kp0;
    vp = vp0;
    const unsigned vs1 = vstep >> 1;
    o = (f32x16){};
    const f32x16 z = {};
#pragma unroll 1
    for (int t = 0; t < 32; ++t) {
      f32x16 s = MFMA32(ldsrd(kp), qf, z);
      const bf16x8 v0 = ldsrd(vp), v1 = ldsrd(vp + 512);
      const float mn = __builtin_fmaxf(m, tile_max(s));
      const float al = __builtin_amdgcn_exp2f(m - mn);
      m = mn;
#pragma unroll
      for (int i = 0; i < 16; ++i) { o[i] *= al; s[i] -= mn; }
      attn_pv(s, v0, v1, o);
      kp += 4096; vp += vs1;
    }
    l = sel(hh, o[0], o[4]);
  }
  float od[8];
#pragma unroll
  for (int i = 0; i < 4; ++i) { od[i] = sel(hh, o[4 + i], o[i]); od[4 + i] = sel(hh, o[8 + i], o[12 + i]); }
  if (w < 4) {
    __builtin_amdgcn_s_setprio(3);
    const int cw = w, ch = cw >> 1;
    const int n = lane & 15, g = lane >> 4, c = n & 7;
    ldsp cb = lds + A_X;
    ldsp kr = cb + ch * C_CH + (KX0 + 97 - 8 * n + 8 * g) * 2;
    ldsp ut = cb + ch * C_CH + 4480 + C_CP * (1 - (c & 1)) + (U0 - C_UO + 97 - 256 + 128 * (n >> 3) + c + (1 - (c & 1)) + 8 * g) * 2;
    const unsigned dsk = *(const unsigned*)(DSK + d0);
    const float ds0 = U2F(dsk << 16), ds1 = U2F(dsk & 0xffff0000u);
    {
      unsigned uv[8];
#pragma unroll
      for (int i = 0; i < 8; ++i) uv[i] = *(const unsigned*)(U + (size_t)(tid + 256 * i) * DM + d0);
#pragma unroll
      for (int i = 0; i < 8; ++i) {
        ldsp p = cb + 4480 + (U0 - C_UO + tid + 256 * i) * 2;
        *(LAS u16*)(p) = (u16)uv[i];
        *(LAS u16*)(p + C_CP + 2) = (u16)uv[i];
        *(LAS u16*)(p + C_CH) = (u16)(uv[i] >> 16);
        *(LAS u16*)(p + C_CH + C_CP + 2) = (u16)(uv[i] >> 16);
      }
    }
    asm volatile("s_waitcnt vmcnt(0) lgkmcnt(0)" ::: "memory");
    cnt_add(lds + A_CNT + 16); cnt_wait(lds + A_CNT + 16, 4);
    f32x4 acc[8] = {};
#pragma unroll 1
    for (int q = 0; q < 4; ++q) conv_class(kr, ut, (cw & 1) + 2 * q, acc);
    const int lo0 = 256 * cw + 128 * (n >> 3) + 32 * g + c;
    float sk0[8], sk1[8];
#pragma unroll
    for (int e = 0; e < 8; ++e) {
      const int l = lo0 + 8 * (e & 3) + 1024 * (e >> 2);
      sk0[e] = rbf(bf2f(*(LAS u16*)(cb + 4480 + (U0 - C_UO + l) * 2)) * ds0);
      sk1[e] = rbf(bf2f(*(LAS u16*)(cb + C_CH + 4480 + (U0 - C_UO + l) * 2)) * ds1);
    }
#pragma unroll
    for (int ph = 0; ph < 2; ++ph) {
      WAITL(); cnt_add(lds + A_CNT + 8); cnt_wait(lds + A_CNT + 8, 8 * ph + 4);
#pragma unroll
      for (int t = 0; t < 4; ++t) *(LAS f32x4*)(cb + ((cw * 4 + t) * 64 + lane) * 16) = acc[4 * ph + t];
      WAITL(); cnt_add(lds + A_CNT + 8); cnt_wait(lds + A_CNT + 8, 8 * ph + 8);
      const f32x4 y0 = *(LAS f32x4*)(cb + (cw * 64 + lane) * 16) + *(LAS f32x4*)(cb + ((4 + cw) * 64 + lane) * 16);
      const f32x4 y1 = *(LAS f32x4*)(cb + ((8 + cw) * 64 + lane) * 16) + *(LAS f32x4*)(cb + ((12 + cw) * 64 + lane) * 16);
#pragma unroll
      for (int e = 0; e < 4; ++e)
        *(unsigned*)(out + (size_t)(lo0 + 8 * e + 1024 * ph) * 1024 + d0) = cvtpk(rbf(y0[e]) + sk0[4 * ph + e], rbf(y1[e]) + sk1[4 * ph + e]);
    }
    WAITL(); cnt_add(lds + A_CNT + 12);
  }
  ldsp sp = lds + A_X + (qt * 128 + lane) * 16;
  ldsp sq = lds + A_ONE + 2048 + (qt * 32 + r) * 8;
  if (kh == 1) {
    cnt_wait(lds + A_CNT + 12, 4);
    *(LAS f32x4*)(sp) = (f32x4){od[0], od[1], od[2], od[3]};
    *(LAS f32x4*)(sp + 1024) = (f32x4){od[4], od[5], od[6], od[7]};
    if (hh == 0) *(LAS f32x2*)(sq) = (f32x2){m, l};
  }
  LBAR();
  if (kh == 0) {
    const f32x4 a = *(LAS f32x4*)(sp), b = *(LAS f32x4*)(sp + 1024);
    const f32x2 ml = *(LAS f32x2*)(sq);
    const float mm = __builtin_fmaxf(m, ml[0]);
    const float s0 = __builtin_amdgcn_exp2f(m - mm), s1 = __builtin_amdgcn_exp2f(ml[0] - mm);
    const float inv = 1.f / (l * s0 + ml[1] * s1);
    const float f0 = s0 * inv, f1 = s1 * inv;
    float q[8];
#pragma unroll
    for (int i = 0; i < 4; ++i) { q[i] = od[i] * f0 + a[i] * f1; q[4 + i] = od[4 + i] * f0 + b[i] * f1; }
    const u32x4 ov = {cvtpk(q[0], q[1]), cvtpk(q[2], q[3]), cvtpk(q[4], q[5]), cvtpk(q[6], q[7])};
    *(u32x4*)((u16*)(ws + WS_O) + (size_t)(qb * 256 + qt * 32 + r) * DM + h * 16 + hh * 8) = ov;
  }
}

constexpr int K3_LDS = 131072;

extern "C" __global__ __launch_bounds__(512, 2) void k3_out(const u16* __restrict__ Wo, const char* __restrict__ ws, u16* __restrict__ out) {
  extern __shared__ __attribute__((aligned(16))) char smem[];
  ldsp lds = (ldsp)smem;
  const int tid = TIDX, lane = tid & 63, w = rfl(tid >> 6), bid = BIDX;
  const unsigned lds0 = (unsigned)(uintptr_t)lds;
  const int xcd = bid & 7, idx = bid >> 3;
  const int mt = xcd * 4 + (idx >> 3), nt = idx & 7, m0 = mt * 64, n0 = nt * 64;
  const int r = lane & 31, hh = lane >> 5, wm = w & 1, wn = (w >> 1) & 1, kk = w >> 2;
  const u16* O = (const u16*)(ws + WS_O);
  const int lrow = w * 8 + (lane >> 3), sl = lane & 7;
  const u16* as = O + (size_t)(m0 + lrow) * DM + ((sl ^ ((lrow >> 1) & 7)) << 3);
  const u16* bs = Wo + (size_t)lrow * DM + n0 + ((sl ^ (((lrow >> 1) & 1) << 2)) << 3);
  const int rot = (idx + (idx >> 3)) & 7;
#pragma unroll
  for (int st = 0; st < 8; ++st) {
    const int ks = ((st + rot) & 7) * 64;
    glds16(as + ks, rfl(lds0 + st * 16384 + w * 1024));
    glds16(bs + (size_t)ks * DM, rfl(lds0 + st * 16384 + 8192 + w * 1024));
  }
  unsigned xa[2], wa;
  {
    const int row = 32 * wm + r;
#pragma unroll
    for (int i = 0; i < 2; ++i) xa[i] = row * 128 + (((4 * kk + 2 * i + hh) ^ ((row >> 1) & 7)) << 4);
    const int q = (lane & 15) >> 2, p = lane & 3, blk = (lane >> 4) & 1;
    const unsigned cb = (unsigned)(64 * wn + 32 * blk + 8 * p) ^ (unsigned)(((q >> 1) & 1) << 6);
    wa = 8192 + (32 * kk + 8 * hh + q) * 128 + cb;
  }
  f32x16 acc = {};
#define K3_COMP(st)                                                                              \
  do {                                                                                           \
    ldsp sl_ = lds + (st) * 16384;                                                               \
    bf16x8 xf[2], wf[2];                                                                         \
    _Pragma("unroll") for (int i = 0; i < 2; ++i) {                                              \
      xf[i] = ldsrd(sl_ + xa[i]);                                                                \
      const s16x4 lo = trrd(sl_ + wa + i * 2048);                                                \
      const s16x4 hi = trrd(sl_ + wa + i * 2048 + 512);                                          \
      wf[i] = __builtin_shufflevector(lo, hi, 0, 1, 2, 3, 4, 5, 6, 7);                           \
    }                                                                                            \
    acc = MFMA32(wf[0], xf[0], acc);                                                             \
    acc = MFMA32(wf[1], xf[1], acc);                                                             \
  } while (0)
  WAITBAR(12); K3_COMP(0); K3_COMP(1);
  WAITBAR(8); K3_COMP(2); K3_COMP(3);
  WAITBAR(4); K3_COMP(4); K3_COMP(5);
  WAITBAR(0); K3_COMP(6); K3_COMP(7);
  if (kk == 1) {
#pragma unroll
    for (int q = 0; q < 4; ++q)
      *(LAS f32x4*)(lds + (((w & 3) * 4 + q) * 64 + lane) * 16) = (f32x4){acc[4 * q], acc[4 * q + 1], acc[4 * q + 2], acc[4 * q + 3]};
  }
  LBAR();
  if (kk == 0) {
    ldsp tp = lds + 32768 + (32 * wm + r) * 144 + (32 * wn + 4 * hh) * 2;
#pragma unroll
    for (int q = 0; q < 4; ++q) {
      const f32x4 t = *(LAS f32x4*)(lds + ((w * 4 + q) * 64 + lane) * 16);
      const u32x2 ov = {cvtpk(acc[4 * q] + t[0], acc[4 * q + 1] + t[1]), cvtpk(acc[4 * q + 2] + t[2], acc[4 * q + 3] + t[3])};
      *(LAS u32x2*)(tp + 16 * q) = ov;
    }
  }
  LBAR();
  {
    const int row = tid >> 3, seg = tid & 7;
    const u32x4 v = *(LAS u32x4*)(lds + 32768 + row * 144 + seg * 16);
    *(u32x4*)(out + (size_t)(m0 + row) * 1024 + 512 + n0 + seg * 8) = v;
  }
#undef K3_COMP
}

#if defined(__HIP_DEVICE_COMPILE__)
#pragma clang attribute pop
#endif

extern "C" void kernel_launch(void* const* d_in, const int* in_sizes, int n_in, void* d_out, int out_size, void* d_ws, size_t ws_size,
                              hipStream_t stream) {
  const u16* u = (const u16*)d_in[0];
  const u16* lar = (const u16*)d_in[1];
  const u16* aim = (const u16*)d_in[2];
  const u16* cr = (const u16*)d_in[3];
  const u16* ci = (const u16*)d_in[4];
  const u16* ldt = (const u16*)d_in[5];
  const u16* dsk = (const u16*)d_in[6];
  const u16* x = (const u16*)d_in[7];
  const u16* wqkv = (const u16*)d_in[8];
  const u16* wo = (const u16*)d_in[9];
  (void)hipFuncSetAttribute((const void*)k1_qkv_kgen, hipFuncAttributeMaxDynamicSharedMemorySize, K1_LDS);
  (void)hipFuncSetAttribute((const void*)k2_attn, hipFuncAttributeMaxDynamicSharedMemorySize, A_LDS);
  (void)hipFuncSetAttribute((const void*)k3_out, hipFuncAttributeMaxDynamicSharedMemorySize, K3_LDS);
  k1_qkv_kgen<<<512, 512, K1_LDS, stream>>>(x, wqkv, u, lar, aim, cr, ci, ldt, (char*)d_ws, wo, dsk, (const u16*)d_out);
  k2_attn<<<256, 1024, A_LDS, stream>>>((char*)d_ws, wo, dsk, (u16*)d_out, u);
  k3_out<<<256, 512, K3_LDS, stream>>>(wo, (const char*)d_ws, (u16*)d_out);
}
```

```cpp
#include <hip/hip_runtime.h>
#include <stdint.h>

#if defined(__HIP_DEVICE_COMPILE__)
#pragma clang attribute push(__attribute__((target("no-packed-fp32-ops"))), apply_to = function)
#define TIDX ((int)__builtin_amdgcn_workitem_id_x())
#define BIDX ((int)__builtin_amdgcn_workgroup_id_x())
#else
#define TIDX 0
#define BIDX 0
#endif
#define U2F(x) __builtin_bit_cast(float, (unsigned)(x))
#define F2U(x) __builtin_bit_cast(unsigned, (float)(x))

typedef unsigned short u16;
typedef __attribute__((ext_vector_type(8))) short bf16x8;
typedef __attribute__((ext_vector_type(4))) short s16x4;
typedef __attribute__((ext_vector_type(16))) float f32x16;
typedef __attribute__((ext_vector_type(4))) float f32x4;
typedef __attribute__((ext_vector_type(2))) float f32x2;
typedef __attribute__((ext_vector_type(4))) unsigned u32x4;
typedef __attribute__((ext_vector_type(2))) unsigned u32x2;
typedef __attribute__((ext_vector_type(2))) __bf16 bf2_t;
#define DI __device__ __forceinline__
#define LAS __attribute__((address_space(3)))
typedef LAS char* ldsp;

#define MFMA32(a, b, c) __builtin_amdgcn_mfma_f32_32x32x16_bf16((a), (b), (c), 0, 0, 0)
#define MFMA16(a, b, c) __builtin_amdgcn_mfma_f32_16x16x32_bf16((a), (b), (c), 0, 0, 0)
#define WAITBAR(N) asm volatile("s_waitcnt vmcnt(" #N ") lgkmcnt(0)\n\ts_barrier" ::: "memory")
#define WAITV(N) asm volatile("s_waitcnt vmcnt(" #N ")" ::: "memory")
#define LBAR() asm volatile("s_waitcnt lgkmcnt(0)\n\ts_barrier" ::: "memory")
#define PIN(x) asm volatile("" : "+v"(x))

DI float bf2f(unsigned x) { return U2F(x << 16); }
DI unsigned cvtpk(float a, float b) { f32x2 v = {a, b}; bf2_t r = __builtin_convertvector(v, bf2_t); return __builtin_bit_cast(unsigned, r); }
DI float rbf(float f) { return U2F(cvtpk(f, 0.f) << 16); }
DI float sel(bool c, float a, float b) { PIN(a); PIN(b); return c ? a : b; }
DI void glds16(const void* g, unsigned lds_base) {
  unsigned sv;
  asm volatile("s_mov_b32 %0, m0\n\ts_mov_b32 m0, %2\n\ts_nop 0\n\tglobal_load_lds_dwordx4 %1, off\n\ts_mov_b32 m0, %0"
               : "=&s"(sv) : "v"(g), "s"(lds_base) : "memory");
}
DI s16x4 trrd(ldsp p) { return __builtin_amdgcn_ds_read_tr16_b64_v4i16((LAS s16x4*)p); }
DI bf16x8 ldsrd(ldsp p) { return *(LAS bf16x8*)p; }
DI unsigned rfl(unsigned x) { return __builtin_amdgcn_readfirstlane(x); }

constexpr int DM = 512, NST = 128;
constexpr size_t WS_Q = 0, WS_K = 2u << 20, WS_V = 4u << 20, WS_O = 6u << 20, WS_KR = 8u << 20;
constexpr int RP = 2560;
constexpr int KX0 = 2079;
constexpr int U0 = 192;
constexpr float QSCALE = 0.25f * 1.4426950408889634f;

constexpr int K1_SLOT = 16384, K1_WOFF = 4096, K1_NS = 5;
constexpr int K1_PSZ = 3 * 256 * 4, K1_PART = 0, K1_PAR = 8 * K1_PSZ, K1_PAR2 = K1_PAR + 4096, K1_DUMP = K1_PAR2 + 1024, K1_TAB = K1_DUMP + 1024;
constexpr int K1_LDS = K1_NS * K1_SLOT;
[[maybe_unused]] constexpr int CODE_KB = 20;

DI int kbase(int T, int i) {
  if (T == 0) return 128 * (i >> 3) + (i & 7);
  if (T == 1) return i < 8 ? 256 + 128 * (i >> 2) + 2 * (i & 3) : 512 + 128 * ((i - 8) >> 1) + 4 * (i & 1);
  return i < 8 ? 1024 + 128 * i : 2048;
}
DI void kgen_state(ldsp pp, ldsp pp2, const float (&ps)[4], float (&bv)[2], float (&av)[3][2]) {
  const f32x4 pr = *(LAS f32x4*)pp;
  const float cr = pr[3], ci = *(LAS float*)pp2;
  float e[4], f[4], c[4], s[4], k[4];
#pragma unroll
  for (int i = 0; i < 4; ++i) { k[i] = pr[1] * ps[i]; e[i] = pr[0] * ps[i]; }
#pragma unroll
  for (int i = 0; i < 4; ++i) { k[i] = __builtin_rintf(k[i]); e[i] = __builtin_amdgcn_exp2f(e[i]); }
#pragma unroll
  for (int i = 0; i < 4; ++i) f[i] = __builtin_fmaf(pr[1], ps[i], -k[i]);
#pragma unroll
  for (int i = 0; i < 4; ++i) f[i] = __builtin_fmaf(pr[2], ps[i], f[i]);
#pragma unroll
  for (int i = 0; i < 4; ++i) { c[i] = __builtin_amdgcn_cosf(f[i]); s[i] = __builtin_amdgcn_sinf(f[i]); }
  bv[0] = e[0] * c[0]; bv[1] = e[0] * s[0];
#pragma unroll
  for (int T = 0; T < 3; ++T) {
    av[T][0] = __builtin_fmaf(-ci, s[T + 1], cr * c[T + 1]) * e[T + 1];
    av[T][1] = __builtin_fmaf(ci, c[T + 1], cr * s[T + 1]) * -e[T + 1];
  }
}
typedef __attribute__((ext_vector_type(2))) _Float16 h2_t;
typedef __attribute__((ext_vector_type(8))) _Float16 h8_t;
DI unsigned cvth(float a, float b) { f32x2 v = {a, b}; return __builtin_bit_cast(unsigned, __builtin_convertvector(v, h2_t)); }
DI void kgen_block(ldsp pp, ldsp pp2, const float (&ps)[4], f32x4 (&ka)[3]) {
  u32x4 bh, ah[3];
#pragma unroll
  for (int q = 0; q < 4; ++q) {
    float bv[2], av[3][2];
    kgen_state(pp + q * 64, pp2 + q * 16, ps, bv, av);
    bh[q] = cvth(bv[0], bv[1]);
#pragma unroll
    for (int T = 0; T < 3; ++T) ah[T][q] = cvth(av[T][0], av[T][1]);
  }
#pragma unroll
  for (int T = 0; T < 3; ++T)
    ka[T] = __builtin_amdgcn_mfma_f32_16x16x32_f16(__builtin_bit_cast(h8_t, ah[T]), __builtin_bit_cast(h8_t, bh), ka[T], 0, 0, 0);
}

template <bool ISV>
DI void k1_gemm_stage(ldsp sl, const unsigned (&xa)[2][2], unsigned wa, f32x16 (&acc)[2], bool issue, const u16* s0, const u16* s1, unsigned dst) {
  bf16x8 xf[2][2], wf[2];
#pragma unroll
  for (int i = 0; i < 2; ++i) {
    xf[0][i] = ldsrd(sl + xa[0][i]);
    xf[1][i] = ldsrd(sl + xa[1][i]);
    const s16x4 lo = trrd(sl + wa + i * 16 * 384), hi = trrd(sl + wa + i * 16 * 384 + 4 * 384);
    wf[i] = __builtin_shufflevector(lo, hi, 0, 1, 2, 3, 4, 5, 6, 7);
  }
  if (issue) {
    glds16(s0, dst);
    glds16(s1, dst + 8192);
  }
#pragma unroll
  for (int i = 0; i < 2; ++i) {
    if (ISV) { acc[0] = MFMA32(xf[0][i], wf[i], acc[0]); acc[1] = MFMA32(xf[1][i], wf[i], acc[1]); }
    else     { acc[0] = MFMA32(wf[i], xf[0][i], acc[0]); acc[1] = MFMA32(wf[i], xf[1][i], acc[1]); }
  }
}
template <bool ISV>
DI void k1_gemm_loop(ldsp lds, unsigned lds0, int w, int rot, const u16* src0, const u16* src1, size_t step0, const unsigned (&xa)[2][2],
                     unsigned wa, f32x16 (&acc)[2]) {
  WAITBAR(6);
  k1_gemm_stage<ISV>(lds, xa, wa, acc, false, src0, src1, 0);
  unsigned so = K1_SLOT, po = 0;
#pragma unroll 1
  for (int st = 1; st < 12; ++st) {
    WAITBAR(6);
    const int ks = (st + K1_NS - 1 + rot) & 15;
    k1_gemm_stage<ISV>(lds + so, xa, wa, acc, true, src0 + ks * step0, src1 + (size_t)ks * (32 * 1536), rfl(lds0 + po + w * 1024));
    po = so;
    so = so == (K1_NS - 1) * K1_SLOT ? 0 : so + K1_SLOT;
  }
  WAITBAR(6); k1_gemm_stage<ISV>(lds + (12 % K1_NS) * K1_SLOT, xa, wa, acc, false, src0, src1, 0);
  WAITBAR(4); k1_gemm_stage<ISV>(lds + (13 % K1_NS) * K1_SLOT, xa, wa, acc, false, src0, src1, 0);
  WAITBAR(2); k1_gemm_stage<ISV>(lds + (14 % K1_NS) * K1_SLOT, xa, wa, acc, false, src0, src1, 0);
  WAITBAR(0); k1_gemm_stage<ISV>(lds + (15 % K1_NS) * K1_SLOT, xa, wa, acc, false, src0, src1, 0);
}
DI const u16* k1_wsrc(const u16* W, int p, int lane, int xcd) {
  const int off = p * 1024 + lane * 16, row = off / 384, ch = (off - row * 384) >> 4;
  const int cs = ch ^ (((row >> 1) & 1) << 2);
  return W + (size_t)row * 1536 + (cs >> 3) * 512 + xcd * 64 + (cs & 7) * 8;
}

DI void k1_gemm(ldsp lds, const u16* __restrict__ X, const u16* __restrict__ W, char* __restrict__ ws) {
  const unsigned lds0 = (unsigned)(uintptr_t)lds;
  const int tid = TIDX, lane = tid & 63, w = rfl(tid >> 6), bid = BIDX - 256;
  const int xcd = bid & 7, mt = bid >> 3;
  const int m0 = mt * 64;
  const int r = lane & 31, hh = lane >> 5;
  const int rot = 2 * (mt & 7);
  const u16* src0;
  size_t step0;
  if (w < 4) {
    const int row = w * 16 + (lane >> 2);
    src0 = X + (size_t)(m0 + row) * DM + (((lane & 3) ^ ((row >> 2) & 3)) << 3);
    step0 = 32;
  } else {
    src0 = k1_wsrc(W, w - 4, lane, xcd);
    step0 = 32 * 1536;
  }
  const u16* src1 = k1_wsrc(W, w + 4, lane, xcd);
  __builtin_amdgcn_s_sleep(8);
#pragma unroll
  for (int st = 0; st < K1_NS; ++st) {
    const int ks = (st + rot) & 15;
    glds16(src0 + ks * step0, rfl(lds0 + st * K1_SLOT + w * 1024));
    glds16(src1 + (size_t)ks * (32 * 1536), rfl(lds0 + st * K1_SLOT + (8 + w) * 1024));
  }
  __builtin_amdgcn_s_setprio(3);
  if (w >= 6) {
    WAITBAR(6);
    unsigned po = 0;
#pragma unroll 1
    for (int st = 1; st < 12; ++st) {
      WAITBAR(6);
      const int ks = (st + K1_NS - 1 + rot) & 15;
      glds16(src0 + ks * step0, rfl(lds0 + po + w * 1024));
      glds16(src1 + (size_t)ks * (32 * 1536), rfl(lds0 + po + (8 + w) * 1024));
      po = po == (K1_NS - 1) * K1_SLOT ? 0 : po + K1_SLOT;
    }
    WAITBAR(6); WAITBAR(4); WAITBAR(2); WAITBAR(0);
    return;
  }
  unsigned xa[2][2];
#pragma unroll
  for (int a = 0; a < 2; ++a) {
    const int row = 32 * a + r;
#pragma unroll
    for (int i = 0; i < 2; ++i) xa[a][i] = row * 64 + (((2 * i + hh) ^ ((row >> 2) & 3)) << 4);
  }
  const int q = (lane & 15) >> 2;
  const unsigned wa = (unsigned)(K1_WOFF + (8 * hh + q) * 384 + ((64 * w) ^ (((q >> 1) & 1) << 6)) + 32 * ((lane >> 4) & 1) + 8 * (lane & 3));
  f32x16 acc[2] = {};
  const int hb = xcd * 4 + (w & 1) * 2;
  if (w >= 4) {
    k1_gemm_loop<true>(lds, lds0, w, rot, src0, src1, step0, xa, wa, acc);
#pragma unroll
    for (int a = 0; a < 2; ++a) {
      const int mtile = mt * 2 + a;
      const int h = hb + (r >> 4), d = r & 15, si = d < 4 ? d : (d < 8 ? d + 8 : d - 4);
      u16* dst = (u16*)(ws + WS_V) + ((((size_t)h * 128 + 2 * mtile) * 2 + hh) * 16 + si) * 8;
#pragma unroll
      for (int s = 0; s < 2; ++s) {
        u32x4 o = {cvtpk(acc[a][8 * s], acc[a][8 * s + 1]), cvtpk(acc[a][8 * s + 2], acc[a][8 * s + 3]),
                   cvtpk(acc[a][8 * s + 4], acc[a][8 * s + 5]), cvtpk(acc[a][8 * s + 6], acc[a][8 * s + 7])};
        *(u32x4*)(dst + s * 256) = o;
      }
    }
  } else {
    k1_gemm_loop<false>(lds, lds0, w, rot, src0, src1, step0, xa, wa, acc);
    const bool isq = w < 2;
    const float sc = isq ? QSCALE : 1.f;
#pragma unroll
    for (int a = 0; a < 2; ++a) {
      const int mtile = mt * 2 + a;
      u16* dst = (u16*)(ws + (isq ? WS_Q : WS_K)) + ((((size_t)hb * 64 + mtile) * 2 + hh) * 32 + r) * 8;
#pragma unroll
      for (int s = 0; s < 2; ++s) {
        u32x4 o = {cvtpk(acc[a][8 * s] * sc, acc[a][8 * s + 1] * sc), cvtpk(acc[a][8 * s + 2] * sc, acc[a][8 * s + 3] * sc),
                   cvtpk(acc[a][8 * s + 4] * sc, acc[a][8 * s + 5] * sc), cvtpk(acc[a][8 * s + 6] * sc, acc[a][8 * s + 7] * sc)};
        *(u32x4*)(dst + (size_t)s * 64 * 512) = o;
      }
    }
  }
}

DI void k1_kgen(ldsp lds, const u16* __restrict__ X, const u16* __restrict__ W, const u16* __restrict__ U, const u16* __restrict__ LAR, const u16* __restrict__ AIM, const u16* __restrict__ CR,
                const u16* __restrict__ CI, const u16* __restrict__ LDT, char* __restrict__ ws, const u16* __restrict__ Wo,
                const u16* __restrict__ DSK, const u16* __restrict__ out) {
  const int tid = TIDX, lane = tid & 63, w = rfl(tid >> 6), bid = BIDX;
  const int r = lane & 31;
  const int cp0 = 2 * ((bid & 7) * 32 + (bid >> 3));
  const int ch = w >> 2, pch = cp0 + ch, pi = pch * NST + (w & 3) * 32 + r;
  unsigned plar = LAR[pi], paim = AIM[pi], pcr = CR[pi], pci = CI[pi], pldt = LDT[pch];
  const int t8 = tid & 255;
  PIN(plar); PIN(paim); PIN(pcr); PIN(pci); PIN(pldt);
  {
    const int xcd = bid & 7, idx = bid >> 3;
    const unsigned dump = (unsigned)(uintptr_t)lds + K1_DUMP;
    if (lane < 48) {
      const int rr = lane / 24, sg = (lane - rr * 24) >> 3, ck = lane & 7;
      glds16((const char*)W + ((size_t)(idx * 16 + w * 2 + rr) * 1536 + sg * 512 + xcd * 64) * 2 + ck * 16, rfl(dump));
    }
  }
#if defined(__HIP_DEVICE_COMPILE__)
  if (w == 7) {
    const char *c1, *c3;
    asm volatile("s_getpc_b64 s[40:41]\n\ts_add_u32 s40, s40, k1_qkv_kgen@rel32@lo+4\n\ts_addc_u32 s41, s41, k1_qkv_kgen@rel32@hi+12\n\ts_mov_b64 %0, s[40:41]"
                 : "=s"(c1) :: "s40", "s41", "scc");
    asm volatile("s_getpc_b64 s[40:41]\n\ts_add_u32 s40, s40, k3_out@rel32@lo+4\n\ts_addc_u32 s41, s41, k3_out@rel32@hi+12\n\ts_mov_b64 %0, s[40:41]"
                 : "=s"(c3) :: "s40", "s41", "scc");
    const int idx = bid >> 3;
    const long span = (c3 - c1) + 2048;
    const char* p = c1 + idx * 1024;
    bool ok = span > 2048 && span < 65536 && (idx + 1) * 1024 <= span;
    if (idx >= CODE_KB) {
      const int j = idx - CODE_KB;
      p = j < 4 ? (const char*)out + j * (1 << 20) : (j < 6 ? ws + WS_O + (j - 4) * (1 << 20) : (j == 6 ? (const char*)Wo : (const char*)DSK));
      ok = j < 8;
    }
    if (ok) glds16(p + lane * 16, rfl((unsigned)(uintptr_t)lds + K1_DUMP));
  }
#endif
  float cunsc;
  {
    const float dt = rbf(__builtin_expf(bf2f(pldt)));
    const float ar = rbf(__builtin_expf(bf2f(plar)));
    const float zi = bf2f(paim) * dt;
    float cr = bf2f(pcr) * dt, ci = bf2f(pci) * dt;
    unsigned ex = F2U(__builtin_fabsf(cr) + __builtin_fabsf(ci)) >> 23;
#pragma unroll
    for (int d = 1; d < 32; d <<= 1) {
      const unsigned o = (unsigned)__builtin_amdgcn_ds_bpermute((lane ^ d) << 2, (int)ex);
      ex = o > ex ? o : ex;
    }
    ex = ex < 1u ? 1u : (ex > 252u ? 252u : ex);
    const float csc = U2F((253u - ex) << 23);
    cunsc = U2F((ex + 1u) << 23);
    cr *= csc; ci *= csc;
    const float HI = 0.15915493667125702f, LO = 6.4206382e-09f;
    const float wh = zi * HI;
    const float wl = __builtin_fmaf(zi, HI, -wh) + zi * LO;
    if (lane < 32) {
      *(LAS f32x4*)(lds + K1_PAR + (w * 32 + lane) * 16) = (f32x4){-(ar * dt) * 1.4426950408889634f, wh, wl, cr};
      *(LAS float*)(lds + K1_PAR2 + (w * 32 + lane) * 4) = ci;
    }
  }
  const int ki = lane & 15, kg = lane >> 4;
  const float ps[4] = {(float)(8 * ki), (float)kbase(0, ki), (float)kbase(1, ki), (float)kbase(2, ki)};
  f32x4 ka[3] = {};
  ldsp pp = lds + K1_PAR + (w * 32 + kg) * 16;
  ldsp pp2 = lds + K1_PAR2 + (w * 32 + kg) * 4;
#pragma unroll 1
  for (int b = 0; b < 2; ++b) kgen_block(pp + b * 256, pp2 + b * 64, ps, ka);
#pragma unroll
  for (int T = 0; T < 3; ++T) *(LAS f32x4*)(lds + K1_PART + w * K1_PSZ + (T * 64 + lane) * 16) = ka[T] * cunsc;
  WAITBAR(0);
  if ((w & 3) < 3) {
    const int T = w & 3;
    f32x4 v = *(LAS f32x4*)(lds + K1_PART + (4 * ch) * K1_PSZ + (T * 64 + lane) * 16);
#pragma unroll
    for (int q = 1; q < 4; ++q) v += *(LAS f32x4*)(lds + K1_PART + (4 * ch + q) * K1_PSZ + (T * 64 + lane) * 16);
#pragma unroll
    for (int j = 0; j < 4; ++j) {
      const int row = 4 * kg + j;
      const int b0 = 128 * (row >> 3) + (row & 7);
      const int b1 = row < 8 ? 256 + 128 * (row >> 2) + 2 * (row & 3) : 128 * (row >> 1) + 4 * (row & 1);
      const int b2 = 1024 + 128 * row;
      const int pos = (T == 0 ? b0 : (T == 1 ? b1 : b2)) + 8 * ki;
      if (pos <= 2048) *(LAS float*)(lds + K1_TAB + (ch * 2052 + pos) * 4) = 2.f * v[j];
    }
  }
  LBAR();
  {
    float v[8];
#pragma unroll
    for (int jj = 0; jj < 8; ++jj) {
      const int pos = (int)rbf((float)(2047 - (t8 * 8 + jj)));
      v[jj] = *(LAS float*)(lds + K1_TAB + (ch * 2052 + pos) * 4);
    }
    u16* kr = (u16*)(ws + WS_KR) + (size_t)pch * RP;
    *(u32x4*)(kr + (4 + t8) * 8) = (u32x4){cvtpk(v[0], v[1]), cvtpk(v[2], v[3]), cvtpk(v[4], v[5]), cvtpk(v[6], v[7])};
    if (t8 < 24) *(u32x4*)(kr + (t8 < 4 ? t8 : 256 + t8) * 8) = (u32x4){0u, 0u, 0u, 0u};
  }
}

extern "C" __global__ __launch_bounds__(512, 4) void k1_qkv_kgen(const u16* __restrict__ X, const u16* __restrict__ W, const u16* __restrict__ U,
                                                      const u16* __restrict__ LAR, const u16* __restrict__ AIM, const u16* __restrict__ CR,
                                                      const u16* __restrict__ CI, const u16* __restrict__ LDT, char* __restrict__ ws,
                                                      const u16* __restrict__ Wo, const u16* __restrict__ DSK, const u16* __restrict__ out) {
  extern __shared__ __attribute__((aligned(16))) char smem[];
  if (BIDX >= 256) k1_gemm((ldsp)smem, X, W, ws);
  else k1_kgen((ldsp)smem, X, W, U, LAR, AIM, CR, CI, LDT, ws, Wo, DSK, out);
}

constexpr int A_ONE = 131072, A_CNT = A_ONE + 256, A_X = A_ONE + 4864, A_LDS = 163840;
constexpr int C_CP = 4480 + 64, C_CH = 4480 + 2 * C_CP, C_UO = 32;

DI void attn_pv(const f32x16& s_in, const bf16x8& v0, const bf16x8& v1, f32x16& o) {
  f32x16 s;
#pragma unroll
  for (int i = 0; i < 8; ++i) s[i] = __builtin_amdgcn_exp2f(s_in[i]);
  const u32x4 p0 = {cvtpk(s[0], s[1]), cvtpk(s[2], s[3]), cvtpk(s[4], s[5]), cvtpk(s[6], s[7])};
  o = MFMA32(v0, __builtin_bit_cast(bf16x8, p0), o);
#pragma unroll
  for (int i = 8; i < 16; ++i) s[i] = __builtin_amdgcn_exp2f(s_in[i]);
  const u32x4 p1 = {cvtpk(s[8], s[9]), cvtpk(s[10], s[11]), cvtpk(s[12], s[13]), cvtpk(s[14], s[15])};
  o = MFMA32(v1, __builtin_bit_cast(bf16x8, p1), o);
}
DI bf16x8 rd4(ldsp p) {
  const u32x4 v = {*(LAS unsigned*)p, *(LAS unsigned*)(p + 4), *(LAS unsigned*)(p + 8), *(LAS unsigned*)(p + 12)};
  return __builtin_bit_cast(bf16x8, v);
}
DI void conv_class(ldsp kr, ldsp ut, int rho, f32x4 (&acc)[8]) {
  kr -= 64 * rho; ut -= 64 * rho;
  bf16x8 F[8];
#pragma unroll
  for (int t = 0; t < 8; ++t) F[t] = rd4(ut + 512 * (t + 1));
  if (rho == 0) {
    const bf16x8 fm = rd4(ut);
#pragma unroll
    for (int t = 0; t < 8; ++t) acc[t] = MFMA16(ldsrd(kr - 512 * (t + 1)), fm, acc[t]);
  }
#pragma unroll
  for (int s = 0; s < 8; ++s) {
    const bf16x8 g = ldsrd(kr - 512 * s);
#pragma unroll
    for (int t = s; t < 8; ++t) acc[t] = MFMA16(g, F[t - s], acc[t]);
  }
}
DI void cnt_add(ldsp cnt) {
  asm volatile("" ::: "memory");
  if ((TIDX & 63) == 0) __hip_atomic_fetch_add((LAS unsigned*)cnt, 1u, __ATOMIC_RELAXED, __HIP_MEMORY_SCOPE_WORKGROUP);
}
DI void cnt_wait(ldsp cnt, unsigned target) {
  while (rfl(*(volatile LAS unsigned*)cnt) < target) __builtin_amdgcn_s_sleep(2);
  asm volatile("" ::: "memory");
}
#define WAITL() asm volatile("s_waitcnt lgkmcnt(0)" ::: "memory")
DI float tile_max(const f32x16& s) {
  float m = __builtin_fmaxf(__builtin_fmaxf(s[0], s[1]), __builtin_fmaxf(s[2], s[3]));
#pragma unroll
  for (int i = 4; i < 16; i += 4) m = __builtin_fmaxf(m, __builtin_fmaxf(__builtin_fmaxf(s[i], s[i + 1]), __builtin_fmaxf(s[i + 2], s[i + 3])));
  const auto sw = __builtin_amdgcn_permlane32_swap(F2U(m), F2U(m), false, false);
  return __builtin_fmaxf(U2F(sw[0]), U2F(sw[1]));
}

extern "C" __global__ __launch_bounds__(1024, 4) void k2_attn(char* __restrict__ ws, const u16* __restrict__ Wo, const u16* __restrict__ DSK,
                                                   u16* __restrict__ out, const u16* __restrict__ U) {
  extern __shared__ __attribute__((aligned(16))) char smem[];
  ldsp lds = (ldsp)smem;
  const unsigned lds0 = (unsigned)(uintptr_t)lds;
  const int tid = TIDX, lane = tid & 63, w = rfl(tid >> 6), bid = BIDX;
  const int h = (bid & 7) * 4 + ((bid >> 3) & 3), qb = bid >> 5;
  const int qt = w & 7, kh = w >> 3, r = lane & 31, hh = lane >> 5;
  bf16x8 qf = *(const bf16x8*)(ws + WS_Q + ((size_t)h * 64 + qb * 8 + qt) * 1024 + lane * 16);
  const bool ldr = w >= 8;
  const char* lsrc = ws + ((w & 2) ? WS_V : WS_K) + (size_t)h * 65536 + (w & 1) * 32768 + ((w >> 2) & 1) * 1024 + lane * 16;
  const unsigned ldst = lds0 + ((w >> 2) & 1) * 4096 + (w & 3) * 1024;
  const int d0 = 2 * ((bid & 7) * 32 + (bid >> 3));
  if (w >= 4) {
    if (w < 8) glds16(lsrc - 1024 + 2048, rfl(ldst - 4096 + 8192));
    else glds16(lsrc, rfl(ldst));
    if (w >= 12) glds16(lsrc + 2048, rfl(ldst + 8192));
  } else if (w == 0) {
    if (lane < 4) *(LAS u32x4*)(lds + A_ONE + (lane & 1) * 512 + (lane >> 1) * 4096) = (u32x4){0x3f803f80u, 0x3f803f80u, 0x3f803f80u, 0x3f803f80u};
    if (lane == 4) *(LAS u32x4*)(lds + A_CNT) = (u32x4){0u, 0u, 0u, 0u};
    if (lane == 5) *(LAS u32x4*)(lds + A_CNT + 16) = (u32x4){0u, 0u, 0u, 0u};
  }
  const int di = r < 4 ? r : (r >= 12 && r < 16 ? r - 8 : (r >= 20 && r < 28 ? r - 12 : -1));
  ldsp const kp0 = lds + kh * 1024 + lane * 16;
  ldsp const vp0 = di >= 0 ? lds + 2048 + kh * 1024 + hh * 256 + di * 16 : lds + A_ONE;
  ldsp kp = kp0, vp = vp0;
  const unsigned vstep = di >= 0 ? 8192u : 0u;
  if (w < 4) {
#pragma unroll
    for (int i = 0; i < 5; ++i) {
      const int c16 = tid + 256 * i;
      const int ch = c16 >= 568, cc = c16 - 568 * ch;
      if (c16 < 1136) *(LAS u32x4*)(lds + A_X + ch * C_CH + 4480 + cc * 16) = (u32x4){0u, 0u, 0u, 0u};
    }
    LBAR();
    const int ch = w >> 1, k = w & 1;
    const char* ks = ws + WS_KR + ((size_t)(d0 + ch) * RP + k * 1024 + lane * 8) * 2;
    const unsigned kd = lds0 + A_X + ch * C_CH + k * 2048;
    glds16(ks, rfl(kd));
    glds16(ks + 1024, rfl(kd + 1024));
    if (k && lane < 24) glds16(ks + 2048, rfl(kd + 2048));
  } else {
    WAITBAR(0);
  }
  PIN(qf);
  if (ldr) {
#pragma unroll 1
    for (int i = 2; i < 8; ++i) glds16(lsrc + i * 2048, rfl(ldst + i * 8192));
  }
  float m;
  f32x16 negm, o = {};
  {
    const f32x16 z = {};
    m = tile_max(MFMA32(ldsrd(kp), qf, z));
  }
#pragma unroll
  for (int i = 0; i < 16; ++i) negm[i] = -m;
  if (w >= 4 && w < 8) __builtin_amdgcn_s_setprio(2); else __builtin_amdgcn_s_setprio(3);
  {
    f32x16 sa = MFMA32(ldsrd(kp), qf, negm);
#pragma unroll 1
    for (int it = 0; it < 16; ++it) {
      const bf16x8 k1 = ldsrd(kp + 4096);
      const bf16x8 va0 = ldsrd(vp), va1 = ldsrd(vp + 512);
      const f32x16 sb = MFMA32(k1, qf, negm);
      const bf16x8 k2 = ldsrd(kp + 8192);
      const bf16x8 vb0 = ldsrd(vp + 4096), vb1 = ldsrd(vp + 4608);
      attn_pv(sa, va0, va1, o);
      sa = MFMA32(k2, qf, negm);
      attn_pv(sb, vb0, vb1, o);
      kp += 8192; vp += vstep;
      if ((0x224Bu >> it) & 1u) {
        if (it == 0) {
          if (ldr) {
            WAITV(0);
            cnt_add(lds + A_CNT);
#pragma unroll 1
            for (int i = 8; i < 16; ++i) glds16(lsrc + i * 2048, rfl(ldst + i * 8192));
          }
          cnt_wait(lds + A_CNT, 8);
        } else if (it == 1) {
          if (ldr) {
            WAITV(0);
            cnt_add(lds + A_CNT + 4);
            glds16((const char*)U + ((size_t)(bid >> 3) * 64 + (w - 8) * 8 + (lane >> 3)) * 1024 + (bid & 7) * 128 + (lane & 7) * 16,
                   rfl(lds0 + A_ONE + 1024));
            glds16((const char*)Wo + ((size_t)(bid >> 3) * 16 + (w - 8) * 2) * 1024 + lane * 16, rfl(lds0 + A_ONE + 1024));
            glds16((const char*)Wo + ((size_t)(bid >> 3) * 16 + (w - 8) * 2 + 1) * 1024 + lane * 16, rfl(lds0 + A_ONE + 1024));
          }
        } else if (it == 3) {
          if (w >= 4) __builtin_amdgcn_s_setprio(2);
        } else if (it == 6) {
          cnt_wait(lds + A_CNT + 4, 8);
        } else if (it == 9) {
          if (w >= 4) __builtin_amdgcn_s_setprio(1);
        } else {
          if (w >= 4) __builtin_amdgcn_s_setprio(0);
        }
      }
    }
  }
  WAITV(0);
  float l = sel(hh, o[0], o[4]);
  float nf = 0.f;
#pragma unroll
  for (int i = 0; i < 16; ++i) nf += o[i] - o[i];
  if (__builtin_expect(__builtin_amdgcn_ballot_w64(!(l + nf <= 1.8446744e19f)) != 0, 0)) {
    kp = kp0;
    vp = vp0;
    const unsigned vs1 = vstep >> 1;
    o = (f32x16){};
    const f32x16 z = {};
#pragma unroll 1
    for (int t = 0; t < 32; ++t) {
      f32x16 s = MFMA32(ldsrd(kp), qf, z);
      const bf16x8 v0 = ldsrd(vp), v1 = ldsrd(vp + 512);
      const float mn = __builtin_fmaxf(m, tile_max(s));
      const float al = __builtin_amdgcn_exp2f(m - mn);
      m = mn;
#pragma unroll
      for (int i = 0; i < 16; ++i) { o[i] *= al; s[i] -= mn; }
      attn_pv(s, v0, v1, o);
      kp += 4096; vp += vs1;
    }
    l = sel(hh, o[0], o[4]);
  }
  float od[8];
#pragma unroll
  for (int i = 0; i < 4; ++i) { od[i] = sel(hh, o[4 + i], o[i]); od[4 + i] = sel(hh, o[8 + i], o[12 + i]); }
  if (w < 4) {
    __builtin_amdgcn_s_setprio(3);
    const int cw = w, ch = cw >> 1;
    const int n = lane & 15, g = lane >> 4, c = n & 7;
    ldsp cb = lds + A_X;
    ldsp kr = cb + ch * C_CH + (KX0 + 97 - 8 * n + 8 * g) * 2;
    ldsp ut = cb + ch * C_CH + 4480 + C_CP * (1 - (c & 1)) + (U0 - C_UO + 97 - 256 + 128 * (n >> 3) + c + (1 - (c & 1)) + 8 * g) * 2;
    const unsigned dsk = *(const unsigned*)(DSK + d0);
    const float ds0 = U2F(dsk << 16), ds1 = U2F(dsk & 0xffff0000u);
    {
      unsigned uv[8];
#pragma unroll
      for (int i = 0; i < 8; ++i) uv[i] = *(const unsigned*)(U + (size_t)(tid + 256 * i) * DM + d0);
#pragma unroll
      for (int i = 0; i < 8; ++i) {
        ldsp p = cb + 4480 + (U0 - C_UO + tid + 256 * i) * 2;
        *(LAS u16*)(p) = (u16)uv[i];
        *(LAS u16*)(p + C_CP + 2) = (u16)uv[i];
        *(LAS u16*)(p + C_CH) = (u16)(uv[i] >> 16);
        *(LAS u16*)(p + C_CH + C_CP + 2) = (u16)(uv[i] >> 16);
      }
    }
    asm volatile("s_waitcnt vmcnt(0) lgkmcnt(0)" ::: "memory");
    cnt_add(lds + A_CNT + 16); cnt_wait(lds + A_CNT + 16, 4);
    f32x4 acc[8] = {};
#pragma unroll 1
    for (int q = 0; q < 4; ++q) conv_class(kr, ut, (cw & 1) + 2 * q, acc);
    const int lo0 = 256 * cw + 128 * (n >> 3) + 32 * g + c;
    float sk0[8], sk1[8];
#pragma unroll
    for (int e = 0; e < 8; ++e) {
      const int l = lo0 + 8 * (e & 3) + 1024 * (e >> 2);
      sk0[e] = rbf(bf2f(*(LAS u16*)(cb + 4480 + (U0 - C_UO + l) * 2)) * ds0);
      sk1[e] = rbf(bf2f(*(LAS u16*)(cb + C_CH + 4480 + (U0 - C_UO + l) * 2)) * ds1);
    }
#pragma unroll
    for (int ph = 0; ph < 2; ++ph) {
      WAITL(); cnt_add(lds + A_CNT + 8); cnt_wait(lds + A_CNT + 8, 8 * ph + 4);
#pragma unroll
      for (int t = 0; t < 4; ++t) *(LAS f32x4*)(cb + ((cw * 4 + t) * 64 + lane) * 16) = acc[4 * ph + t];
      WAITL(); cnt_add(lds + A_CNT + 8); cnt_wait(lds + A_CNT + 8, 8 * ph + 8);
      const f32x4 y0 = *(LAS f32x4*)(cb + (cw * 64 + lane) * 16) + *(LAS f32x4*)(cb + ((4 + cw) * 64 + lane) * 16);
      const f32x4 y1 = *(LAS f32x4*)(cb + ((8 + cw) * 64 + lane) * 16) + *(LAS f32x4*)(cb + ((12 + cw) * 64 + lane) * 16);
#pragma unroll
      for (int e = 0; e < 4; ++e)
        *(unsigned*)(out + (size_t)(lo0 + 8 * e + 1024 * ph) * 1024 + d0) = cvtpk(rbf(y0[e]) + sk0[4 * ph + e], rbf(y1[e]) + sk1[4 * ph + e]);
    }
    WAITL(); cnt_add(lds + A_CNT + 12);
  }
  ldsp sp = lds + A_X + (qt * 128 + lane) * 16;
  ldsp sq = lds + A_ONE + 2048 + (qt * 32 + r) * 8;
  if (kh == 1) {
    cnt_wait(lds + A_CNT + 12, 4);
    *(LAS f32x4*)(sp) = (f32x4){od[0], od[1], od[2], od[3]};
    *(LAS f32x4*)(sp + 1024) = (f32x4){od[4], od[5], od[6], od[7]};
    if (hh == 0) *(LAS f32x2*)(sq) = (f32x2){m, l};
  }
  LBAR();
  if (kh == 0) {
    const f32x4 a = *(LAS f32x4*)(sp), b = *(LAS f32x4*)(sp + 1024);
    const f32x2 ml = *(LAS f32x2*)(sq);
    const float mm = __builtin_fmaxf(m, ml[0]);
    const float s0 = __builtin_amdgcn_exp2f(m - mm), s1 = __builtin_amdgcn_exp2f(ml[0] - mm);
    const float inv = 1.f / (l * s0 + ml[1] * s1);
    const float f0 = s0 * inv, f1 = s1 * inv;
    float q[8];
#pragma unroll
    for (int i = 0; i < 4; ++i) { q[i] = od[i] * f0 + a[i] * f1; q[4 + i] = od[4 + i] * f0 + b[i] * f1; }
    const u32x4 ov = {cvtpk(q[0], q[1]), cvtpk(q[2], q[3]), cvtpk(q[4], q[5]), cvtpk(q[6], q[7])};
    *(u32x4*)((u16*)(ws + WS_O) + (size_t)(qb * 256 + qt * 32 + r) * DM + h * 16 + hh * 8) = ov;
  }
}

constexpr int K3_LDS = 131072;

extern "C" __global__ __launch_bounds__(512, 2) void k3_out(const u16* __restrict__ Wo, const char* __restrict__ ws, u16* __restrict__ out) {
  extern __shared__ __attribute__((aligned(16))) char smem[];
  ldsp lds = (ldsp)smem;
  const int tid = TIDX, lane = tid & 63, w = rfl(tid >> 6), bid = BIDX;
  const unsigned lds0 = (unsigned)(uintptr_t)lds;
  const int xcd = bid & 7, idx = bid >> 3;
  const int mt = xcd * 4 + (idx >> 3), nt = idx & 7, m0 = mt * 64, n0 = nt * 64;
  const int r = lane & 31, hh = lane >> 5, wm = w & 1, wn = (w >> 1) & 1, kk = w >> 2;
  const u16* O = (const u16*)(ws + WS_O);
  const int lrow = w * 8 + (lane >> 3), sl = lane & 7;
  const u16* as = O + (size_t)(m0 + lrow) * DM + ((sl ^ ((lrow >> 1) & 7)) << 3);
  const u16* bs = Wo + (size_t)lrow * DM + n0 + ((sl ^ (((lrow >> 1) & 1) << 2)) << 3);
  const int rot = (idx + (idx >> 3)) & 7;
#pragma unroll
  for (int st = 0; st < 8; ++st) {
    const int ks = ((st + rot) & 7) * 64;
    glds16(as + ks, rfl(lds0 + st * 16384 + w * 1024));
    glds16(bs + (size_t)ks * DM, rfl(lds0 + st * 16384 + 8192 + w * 1024));
  }
  unsigned xa[2], wa;
  {
    const int row = 32 * wm + r;
#pragma unroll
    for (int i = 0; i < 2; ++i) xa[i] = row * 128 + (((4 * kk + 2 * i + hh) ^ ((row >> 1) & 7)) << 4);
    const int q = (lane & 15) >> 2, p = lane & 3, blk = (lane >> 4) & 1;
    const unsigned cb = (unsigned)(64 * wn + 32 * blk + 8 * p) ^ (unsigned)(((q >> 1) & 1) << 6);
    wa = 8192 + (32 * kk + 8 * hh + q) * 128 + cb;
  }
  f32x16 acc = {};
#define K3_COMP(st)                                                                              \
  do {                                                                                           \
    ldsp sl_ = lds + (st) * 16384;                                                               \
    bf16x8 xf[2], wf[2];                                                                         \
    _Pragma("unroll") for (int i = 0; i < 2; ++i) {                                              \
      xf[i] = ldsrd(sl_ + xa[i]);                                                                \
      const s16x4 lo = trrd(sl_ + wa + i * 2048);                                                \
      const s16x4 hi = trrd(sl_ + wa + i * 2048 + 512);                                          \
      wf[i] = __builtin_shufflevector(lo, hi, 0, 1, 2, 3, 4, 5, 6, 7);                           \
    }                                                                                            \
    acc = MFMA32(wf[0], xf[0], acc);                                                             \
    acc = MFMA32(wf[1], xf[1], acc);                                                             \
  } while (0)
  WAITBAR(12); K3_COMP(0); K3_COMP(1);
  WAITBAR(8); K3_COMP(2); K3_COMP(3);
  WAITBAR(4); K3_COMP(4); K3_COMP(5);
  WAITBAR(0); K3_COMP(6); K3_COMP(7);
  if (kk == 1) {
#pragma unroll
    for (int q = 0; q < 4; ++q)
      *(LAS f32x4*)(lds + (((w & 3) * 4 + q) * 64 + lane) * 16) = (f32x4){acc[4 * q], acc[4 * q + 1], acc[4 * q + 2], acc[4 * q + 3]};
  }
  LBAR();
  if (kk == 0) {
    ldsp tp = lds + 32768 + (32 * wm + r) * 144 + (32 * wn + 4 * hh) * 2;
#pragma unroll
    for (int q = 0; q < 4; ++q) {
      const f32x4 t = *(LAS f32x4*)(lds + ((w * 4 + q) * 64 + lane) * 16);
      const u32x2 ov = {cvtpk(acc[4 * q] + t[0], acc[4 * q + 1] + t[1]), cvtpk(acc[4 * q + 2] + t[2], acc[4 * q + 3] + t[3])};
      *(LAS u32x2*)(tp + 16 * q) = ov;
    }
  }
  LBAR();
  {
    const int row = tid >> 3, seg = tid & 7;
    const u32x4 v = *(LAS u32x4*)(lds + 32768 + row * 144 + seg * 16);
    *(u32x4*)(out + (size_t)(m0 + row) * 1024 + 512 + n0 + seg * 8) = v;
  }
#undef K3_COMP
}

#if defined(__HIP_DEVICE_COMPILE__)
#pragma clang attribute pop
#endif

extern "C" void kernel_launch(void* const* d_in, const int* in_sizes, int n_in, void* d_out, int out_size, void* d_ws, size_t ws_size,
                              hipStream_t stream) {
  const u16* u = (const u16*)d_in[0];
  const u16* lar = (const u16*)d_in[1];
  const u16* aim = (const u16*)d_in[2];
  const u16* cr = (const u16*)d_in[3];
  const u16* ci = (const u16*)d_in[4];
  const u16* ldt = (const u16*)d_in[5];
  const u16* dsk = (const u16*)d_in[6];
  const u16* x = (const u16*)d_in[7];
  const u16* wqkv = (const u16*)d_in[8];
  const u16* wo = (const u16*)d_in[9];
  (void)hipFuncSetAttribute((const void*)k1_qkv_kgen, hipFuncAttributeMaxDynamicSharedMemorySize, K1_LDS);
  (void)hipFuncSetAttribute((const void*)k2_attn, hipFuncAttributeMaxDynamicSharedMemorySize, A_LDS);
  (void)hipFuncSetAttribute((const void*)k3_out, hipFuncAttributeMaxDynamicSharedMemorySize, K3_LDS);
  k1_qkv_kgen<<<512, 512, K1_LDS, stream>>>(x, wqkv, u, lar, aim, cr, ci, ldt, (char*)d_ws, wo, dsk, (const u16*)d_out);
  k2_attn<<<256, 1024, A_LDS, stream>>>((char*)d_ws, wo, dsk, (u16*)d_out, u);
  k3_out<<<256, 512, K3_LDS, stream>>>(wo, (const char*)d_ws, (u16*)d_out);
}
```
